```python
import torch

def _softmax(x, axis=-1):
    return torch.softmax(x, dim=axis)
def _gelu(x):
    return torch.nn.functional.gelu(x, approximate="tanh")
def _silu(x):
    return torch.nn.functional.silu(x)
def _sigmoid(x):
    return torch.sigmoid(x)
def _layernorm(x, axis=-1, eps=1e-5):
    m = x.mean(dim=axis, keepdim=True)
    v = x.var(dim=axis, keepdim=True, unbiased=False)
    return (x - m) / torch.sqrt(v + eps)
def _scatter_add(tgt, idx, src, axis=0):
    return tgt.clone().index_add_(axis, idx, src)
def _segment_sum(src, seg, n):
    out = torch.zeros((n,) + src.shape[1:], dtype=src.dtype)
    return out.index_add_(0, seg, src)
def _pad(x, pw):
    flat = tuple(v for pair in reversed(pw) for v in pair)
    return torch.nn.functional.pad(x, flat)

kernel_name = 'concat_heads__gqa_block_cached__gqa_block_cached__near_ridge'

def setup_inputs():

    _w = (lambda a: (lambda t: t.to(torch.bfloat16) if t.is_floating_point() else t)(torch.as_tensor(a)))
    def _A_setup():
        B, Sq, D = 8, 112, 576
        Hq, Hkv, Sk, Dh = 32, 16, 512, 128
        assert Hq % Hkv == 0, (Hq, Hkv)
        return {
            "x": _w((lambda *s: torch.randn(*s, dtype=torch.bfloat16) * 0.8)(B, Sq, D)),
            "k_cache": _w((lambda *s: torch.randn(*s, dtype=torch.bfloat16) * 0.8)(B, Hkv, Sk, Dh)),
            "v_cache": _w((lambda *s: torch.randn(*s, dtype=torch.bfloat16) * 0.05)(B, Hkv, Sk, Dh)),
            "wq": _w((lambda *s: torch.randn(*s, dtype=torch.bfloat16) * 0.8)(D, Hq * Dh) * (D ** -0.5)),
            "wo": _w((lambda *s: torch.randn(*s, dtype=torch.bfloat16) * 0.05)(Hq * Dh, D) * ((Hq * Dh) ** -0.5)),
        }
    _A_in = _A_setup()

    def _B_setup():
        B, Sq, D = 8, 112, 576
        Hq, Hkv, Sk, Dh = 32, 16, 512, 128
        assert Hq % Hkv == 0, (Hq, Hkv)
        return {
            "x": _w((lambda *s: torch.randn(*s, dtype=torch.bfloat16) * 0.8)(B, Sq, D)),
            "k_cache": _w((lambda *s: torch.randn(*s, dtype=torch.bfloat16) * 0.8)(B, Hkv, Sk, Dh)),
            "v_cache": _w((lambda *s: torch.randn(*s, dtype=torch.bfloat16) * 0.05)(B, Hkv, Sk, Dh)),
            "wq": _w((lambda *s: torch.randn(*s, dtype=torch.bfloat16) * 0.8)(D, Hq * Dh) * (D ** -0.5)),
            "wo": _w((lambda *s: torch.randn(*s, dtype=torch.bfloat16) * 0.05)(Hq * Dh, D) * ((Hq * Dh) ** -0.5)),
        }
    _B_in = _B_setup()

    return {
        "A_x": _A_in["x"],
        "A_k_cache": _A_in["k_cache"],
        "A_v_cache": _A_in["v_cache"],
        "A_wq": _A_in["wq"],
        "A_wo": _A_in["wo"],
        "B_x": _B_in["x"],
        "B_k_cache": _B_in["k_cache"],
        "B_v_cache": _B_in["v_cache"],
        "B_wq": _B_in["wq"],
        "B_wo": _B_in["wo"],
    }

def reference(A_x, A_k_cache, A_v_cache, A_wq, A_wo, B_x, B_k_cache, B_v_cache, B_wq, B_wo):
    def _A_ref(x, k_cache, v_cache, wq, wo):
        B, Sq, D = x.shape
        Hq, Dh = 32, 128
        Hkv = k_cache.shape[1]
        rep = Hq // Hkv
        q = (lambda x, *d: x.permute(*d))((x @ wq).reshape(B, Sq, Hq, Dh), 0, 2, 1, 3)
        k = (lambda x, n, axis: torch.repeat_interleave(x, n, dim=axis))(k_cache, rep, axis=1)
        v = (lambda x, n, axis: torch.repeat_interleave(x, n, dim=axis))(v_cache, rep, axis=1)
        scores = torch.einsum("bhqd,bhkd->bhqk", q, k) * (Dh ** -0.5)
        p = _softmax(scores, axis=-1)
        o = torch.einsum("bhqk,bhkd->bhqd", p, v)
        return (lambda x, *d: x.permute(*d))(o, 0, 2, 1, 3).reshape(B, Sq, Hq * Dh) @ wo

    def _B_ref(x, k_cache, v_cache, wq, wo):
        B, Sq, D = x.shape
        Hq, Dh = 32, 128
        Hkv = k_cache.shape[1]
        rep = Hq // Hkv
        q = (lambda x, *d: x.permute(*d))((x @ wq).reshape(B, Sq, Hq, Dh), 0, 2, 1, 3)
        k = (lambda x, n, axis: torch.repeat_interleave(x, n, dim=axis))(k_cache, rep, axis=1)
        v = (lambda x, n, axis: torch.repeat_interleave(x, n, dim=axis))(v_cache, rep, axis=1)
        scores = torch.einsum("bhqd,bhkd->bhqk", q, k) * (Dh ** -0.5)
        p = _softmax(scores, axis=-1)
        o = torch.einsum("bhqk,bhkd->bhqd", p, v)
        return (lambda x, *d: x.permute(*d))(o, 0, 2, 1, 3).reshape(B, Sq, Hq * Dh) @ wo

    a = _A_ref(A_x, A_k_cache, A_v_cache, A_wq, A_wo)
    b = _B_ref(B_x, B_k_cache, B_v_cache, B_wq, B_wo)
    return torch.concatenate([a, b], axis=-1)
```

```cpp
#include <hip/hip_runtime.h>
#include <hip/hip_bf16.h>
#include <stdint.h>
#include <utility>

typedef short bf16x8 __attribute__((ext_vector_type(8)));
typedef short s16x4 __attribute__((ext_vector_type(4)));
typedef float f32x16 __attribute__((ext_vector_type(16)));
typedef float f32x4 __attribute__((ext_vector_type(4)));
typedef float f32x2 __attribute__((ext_vector_type(2)));
typedef __bf16 bf16x2_t __attribute__((ext_vector_type(2)));
typedef unsigned u32x4 __attribute__((ext_vector_type(4)));
typedef unsigned u32x2 __attribute__((ext_vector_type(2)));
typedef __attribute__((address_space(3))) const char* lds_cptr;
typedef __attribute__((address_space(3))) char* lds_ptr;
typedef short v4i16_t __attribute__((ext_vector_type(4)));

#define DI __device__ __forceinline__
#define SBAR() __builtin_amdgcn_sched_barrier(0)
#define MFMA(a, b, c) __builtin_amdgcn_mfma_f32_32x32x16_bf16((a), (b), (c), 0, 0, 0)

constexpr int NB = 8, SQ = 112, DM = 576, HQ = 32, HKV = 16, SK = 512, DH = 128;
constexpr int NQ = HQ * DH;
constexpr float QSCALE = 0.088388347648318447f * 1.4426950408889634f;
constexpr float THR = 8.0f;

DI unsigned cvtpk(float lo, float hi) {
  f32x2 v = {lo, hi};
  bf16x2_t b = __builtin_convertvector(v, bf16x2_t);
  return __builtin_bit_cast(unsigned, b);
}
DI bf16x8 pack8(const f32x16& x, int s) {
  u32x4 w = {cvtpk(x[8 * s + 0], x[8 * s + 1]), cvtpk(x[8 * s + 2], x[8 * s + 3]),
             cvtpk(x[8 * s + 4], x[8 * s + 5]), cvtpk(x[8 * s + 6], x[8 * s + 7])};
  return __builtin_bit_cast(bf16x8, w);
}
DI bf16x8 lds_rd128(lds_cptr p) { return *(const __attribute__((address_space(3))) bf16x8*)p; }
DI s16x4 lds_rdtr(lds_cptr p) {
  return __builtin_bit_cast(s16x4, __builtin_amdgcn_ds_read_tr16_b64_v4i16((__attribute__((address_space(3))) v4i16_t*)p));
}
DI bf16x8 cat8(s16x4 lo, s16x4 hi) { return __builtin_shufflevector(lo, hi, 0, 1, 2, 3, 4, 5, 6, 7); }

template <int OFF>
DI void dma16(unsigned m0v, unsigned voff, __amdgpu_buffer_rsrc_t srd, unsigned soff) {
  unsigned keep;
  asm volatile("s_mov_b32 %0, m0\n\ts_mov_b32 m0, %1\n\ts_nop 0\n\t"
               "buffer_load_dwordx4 %2, %3, %4 offen offset:%5 lds\n\ts_mov_b32 m0, %0"
               : "=&s"(keep) : "s"(m0v), "v"(voff), "s"(srd), "s"(soff), "i"(OFF) : "memory");
}
template <int N> DI void wait_vm_bar() { asm volatile("s_waitcnt vmcnt(%0) lgkmcnt(0)\n\ts_barrier" :: "i"(N) : "memory"); }
template <int N> DI void wait_vm() { asm volatile("s_waitcnt vmcnt(%0)" :: "i"(N) : "memory"); }
DI void wait_lgkm0() { asm volatile("s_waitcnt lgkmcnt(0)" ::: "memory"); }

DI __amdgpu_buffer_rsrc_t make_srd(const void* base, unsigned bytes) {
  return __builtin_amdgcn_make_buffer_rsrc((void*)base, (short)0, (int)bytes, 0x00020000);
}

constexpr int A_STAGES = 18;
constexpr int A_SLOT = 24576, A_BASE = 65536, A_X = 0, A_W = 8192;
constexpr int KV_TILES = 8;
constexpr int K1_LDS = 163840;
constexpr int kv_slot_off(int t) {
  return (t % 5) == 0 ? 0 : (t % 5) == 1 ? 32768 : (t % 5) == 2 ? 131072 : (t % 5) == 3 ? 65536 : 98304;
}
constexpr int kv_cnt(int s) { return (s >= 0 && s <= 14 && (s % 2) == 0) ? 1 : (s == 16 ? 4 : 0); }
constexpr int a_cnt(int s) { return (s >= 0 && s < A_STAGES) ? 3 : 0; }
constexpr int a_wait(int s) {
  return s == 0 ? 6
       : s == 1 ? 3 + a_cnt(3) + kv_cnt(0)
       : s == 2 ? a_cnt(3) + kv_cnt(0) + a_cnt(4) + kv_cnt(1)
       : kv_cnt(s - 3) + a_cnt(s + 1) + kv_cnt(s - 2) + a_cnt(s + 2) + kv_cnt(s - 1);
}
constexpr int b_wait(int t) { return t == 0 ? 5 : t == 1 ? 12 : t <= 5 ? 8 : t == 6 ? 4 : 0; }

struct K1Ctx {
  __amdgpu_buffer_rsrc_t sx, sw, sk, sv;
  unsigned ldsb, wid;
  unsigned vx, vw0, vw1, vk0, vk1, vv;
};

template <int S> DI void k1_issue_a(const K1Ctx& c) {
  if constexpr (S < A_STAGES) {
    const unsigned sb = c.ldsb + A_BASE + (S % 4) * A_SLOT;
    dma16<0>(sb + A_X + c.wid * 1024, c.vx, c.sx, S * 64);
    dma16<0>(sb + A_W + c.wid * 2048, c.vw0, c.sw, S * 32 * 8192);
    dma16<0>(sb + A_W + c.wid * 2048 + 1024, c.vw1, c.sw, S * 32 * 8192);
  }
}
template <int T, int P> DI void k1_issue_kv_piece(const K1Ctx& c) {
  const unsigned sb = c.ldsb + kv_slot_off(T);
  if constexpr (P == 0) dma16<0>(sb + c.wid * 1024, c.vk0, c.sk, T * 16384);
  if constexpr (P == 1) dma16<0>(sb + 8192 + c.wid * 1024, c.vk1, c.sk, T * 16384);
  if constexpr (P == 2) dma16<0>(sb + 16384 + c.wid * 2048, c.vv, c.sv, T * 16384);
  if constexpr (P == 3) dma16<1024>(sb + 16384 + c.wid * 2048, c.vv, c.sv, T * 16384);
}
template <int T> DI void k1_issue_kv(const K1Ctx& c) {
  k1_issue_kv_piece<T, 0>(c); k1_issue_kv_piece<T, 1>(c); k1_issue_kv_piece<T, 2>(c); k1_issue_kv_piece<T, 3>(c);
}

template <int S>
DI void k1_stage_a(const K1Ctx& c, f32x16 (&qa)[4], lds_cptr (&xb)[2], lds_cptr (&wb)[4]) {
  wait_vm_bar<a_wait(S)>();
  k1_issue_a<S + 3>(c);
  if constexpr (S <= 14 && (S % 2) == 0) k1_issue_kv_piece<(S / 2) / 4, (S / 2) % 4>(c);
  if constexpr (S == 16) k1_issue_kv<2>(c);
  constexpr int so = A_BASE + (S % 4) * A_SLOT;
#pragma unroll
  for (int ks = 0; ks < 2; ++ks) {
    const bf16x8 xf = lds_rd128(xb[ks] + so + A_X);
#pragma unroll
    for (int dt = 0; dt < 4; ++dt) {
      const s16x4 lo = lds_rdtr(wb[dt] + so + A_W + ks * 8192);
      const s16x4 hi = lds_rdtr(wb[dt] + so + A_W + ks * 8192 + 2048);
      qa[dt] = MFMA(cat8(lo, hi), xf, qa[dt]);
    }
  }
}
template <int... Is>
DI void k1_phase_a(const K1Ctx& c, f32x16 (&qa)[4], lds_cptr (&xb)[2], lds_cptr (&wb)[4], std::integer_sequence<int, Is...>) {
  (k1_stage_a<Is>(c, qa, xb, wb), ...);
}

DI float half_max(float x) {
  auto rr = __builtin_amdgcn_permlane32_swap(__float_as_uint(x), __float_as_uint(x), false, false);
  return fmaxf(__uint_as_float(rr[0]), __uint_as_float(rr[1]));
}
DI float half_sum(float x) {
  auto rr = __builtin_amdgcn_permlane32_swap(__float_as_uint(x), __float_as_uint(x), false, false);
  return __uint_as_float(rr[0]) + __uint_as_float(rr[1]);
}

struct AttnState {
  f32x16 o[4];
  f32x16 negm;
  float mref, l;
};

template <int T>
DI void k1_step_b(const K1Ctx& c, AttnState& st, const bf16x8 (&q)[8], lds_cptr (&kb_)[4], lds_cptr (&vb_)[4]) {
  wait_vm_bar<b_wait(T)>();
  if constexpr (T == 0) { k1_issue_kv<3>(c); k1_issue_kv<4>(c); }
  if constexpr (T >= 2 && T <= 4) k1_issue_kv<T + 3>(c);
  constexpr int so = kv_slot_off(T);
  f32x16 s[2];
#pragma unroll
  for (int kb = 0; kb < 2; ++kb) {
#pragma unroll
    for (int ds = 0; ds < 8; ++ds) {
      const bf16x8 kf = lds_rd128(kb_[ds & 3] + so + (ds >> 2) * 8192 + kb * 4096);
      if (ds == 0) {
        if constexpr (T == 0) { f32x16 z = {}; s[kb] = MFMA(kf, q[ds], z); }
        else s[kb] = MFMA(kf, q[ds], st.negm);
      } else s[kb] = MFMA(kf, q[ds], s[kb]);
    }
  }
  float rm = s[0][0];
#pragma unroll
  for (int r = 1; r < 16; ++r) rm = fmaxf(rm, s[0][r]);
#pragma unroll
  for (int r = 0; r < 16; ++r) rm = fmaxf(rm, s[1][r]);
  rm = half_max(rm);
  if constexpr (T == 0) {
    st.mref = rm;
#pragma unroll
    for (int r = 0; r < 16; ++r) { s[0][r] -= rm; s[1][r] -= rm; st.negm[r] = -rm; }
  } else {
    if (__builtin_expect(__any(rm > THR), 0)) {
      const float dl = fmaxf(rm, 0.f);
      st.mref += dl;
      const float f = __builtin_amdgcn_exp2f(-dl);
      st.l *= f;
#pragma unroll
      for (int r = 0; r < 16; ++r) { s[0][r] -= dl; s[1][r] -= dl; st.negm[r] = -st.mref; }
#pragma unroll
      for (int dt = 0; dt < 4; ++dt)
#pragma unroll
        for (int r = 0; r < 16; ++r) st.o[dt][r] *= f;
    }
  }
  float ps = 0.f;
#pragma unroll
  for (int kb = 0; kb < 2; ++kb)
#pragma unroll
    for (int r = 0; r < 16; ++r) { s[kb][r] = __builtin_amdgcn_exp2f(s[kb][r]); ps += s[kb][r]; }
  st.l += ps;
#pragma unroll
  for (int kb = 0; kb < 2; ++kb)
#pragma unroll
    for (int sx = 0; sx < 2; ++sx) {
      const bf16x8 pf = pack8(s[kb], sx);
#pragma unroll
      for (int dt = 0; dt < 4; ++dt) {
        const s16x4 lo = lds_rdtr(vb_[dt] + so + 16384 + (32 * kb + 16 * sx) * 256);
        const s16x4 hi = lds_rdtr(vb_[dt] + so + 16384 + (32 * kb + 16 * sx + 8) * 256);
        st.o[dt] = MFMA(cat8(lo, hi), pf, st.o[dt]);
      }
    }
}
template <int... Is>
DI void k1_phase_b(const K1Ctx& c, AttnState& st, const bf16x8 (&q)[8], lds_cptr (&kb_)[4], lds_cptr (&vb_)[4],
                   std::integer_sequence<int, Is...>) {
  (k1_step_b<Is>(c, st, q, kb_, vb_), ...);
}

__global__ __launch_bounds__(512, 2) void k1_attn(const __hip_bfloat16* __restrict__ xA, const __hip_bfloat16* __restrict__ kA,
                                                  const __hip_bfloat16* __restrict__ vA, const __hip_bfloat16* __restrict__ wqA,
                                                  const __hip_bfloat16* __restrict__ xB, const __hip_bfloat16* __restrict__ kB,
                                                  const __hip_bfloat16* __restrict__ vB, const __hip_bfloat16* __restrict__ wqB,
                                                  u32x4* __restrict__ ofrag) {
  extern __shared__ __attribute__((aligned(16))) char lds[];
  const int tid = threadIdx.x, lane = tid & 63;
  const unsigned wid = __builtin_amdgcn_readfirstlane(tid >> 6);
  const int bid = blockIdx.x, xcd = bid & 7, idx = bid >> 3;
  const int p = xcd >> 2, kvh = (xcd & 3) * 4 + (idx & 3), b = idx >> 2;
  const int hh = wid >> 2, rb = wid & 3;
  const int r = lane & 31, h = lane >> 5, blk = (lane >> 4) & 1, qq = (lane & 15) >> 2, pp = lane & 3;

  K1Ctx c;
  c.ldsb = (unsigned)(uintptr_t)lds;
  c.wid = wid;
  {
    const __hip_bfloat16* x = p ? xB : xA; const __hip_bfloat16* wq = p ? wqB : wqA;
    const __hip_bfloat16* kc = p ? kB : kA; const __hip_bfloat16* vc = p ? vB : vA;
    c.sx = make_srd(x + (size_t)b * SQ * DM, SQ * DM * 2);
    c.sw = make_srd(wq + kvh * 256, (DM * NQ - kvh * 256) * 2);
    c.sk = make_srd(kc + (size_t)(b * HKV + kvh) * SK * DH, SK * DH * 2);
    c.sv = make_srd(vc + (size_t)(b * HKV + kvh) * SK * DH, SK * DH * 2);
  }
  {
    const unsigned xr = 16 * wid + (lane >> 2);
    c.vx = xr * (DM * 2) + (((lane & 3) ^ ((xr >> 2) & 3)) << 4);
    const unsigned k0 = 4 * wid + (lane >> 5), k1 = k0 + 2;
    c.vw0 = k0 * 8192 + (((lane & 31) ^ ((k0 & 3) << 2)) << 4);
    c.vw1 = k1 * 8192 + (((lane & 31) ^ ((k1 & 3) << 2)) << 4);
    const unsigned kr = 8 * wid + (lane >> 3);
    c.vk0 = kr * 256 + (((lane & 7) ^ ((kr >> 1) & 7)) << 4);
    c.vk1 = c.vk0 + 128;
    const unsigned vr = 8 * wid + (lane >> 4);
    c.vv = vr * 256 + (((lane & 15) ^ ((vr & 3) << 2)) << 4);
  }
  k1_issue_a<0>(c); k1_issue_a<1>(c); k1_issue_a<2>(c);

  const lds_cptr L = (lds_cptr)lds;
  const int xrow = rb * 32 + r;
  lds_cptr xb[2];
  xb[0] = L + xrow * 64 + ((h ^ ((xrow >> 2) & 3)) << 4);
  xb[1] = L + xrow * 64 + (((2 + h) ^ ((xrow >> 2) & 3)) << 4);
  lds_cptr wb[4];
  {
    const int psw = ((pp & 1) << 1) | (pp >> 1);
#pragma unroll
    for (int dt = 0; dt < 4; ++dt) wb[dt] = L + (8 * h + qq) * 512 + hh * 256 + ((dt ^ qq) << 6) + blk * 32 + psw * 8;
  }
  f32x16 qa[4] = {};
  k1_phase_a(c, qa, xb, wb, std::make_integer_sequence<int, A_STAGES>{});

  bf16x8 q[8];
#pragma unroll
  for (int dt = 0; dt < 4; ++dt) {
#pragma unroll
    for (int i = 0; i < 16; ++i) qa[dt][i] *= QSCALE;
    q[2 * dt] = pack8(qa[dt], 0);
    q[2 * dt + 1] = pack8(qa[dt], 1);
  }

  lds_cptr kb_[4], vb_[4];
#pragma unroll
  for (int j = 0; j < 4; ++j) {
    kb_[j] = L + r * 128 + (((2 * j + h) ^ ((r >> 1) & 7)) << 4);
    vb_[j] = L + (4 * h + qq) * 256 + ((j ^ qq) << 6) + blk * 32 + pp * 8;
  }
  AttnState st;
#pragma unroll
  for (int dt = 0; dt < 4; ++dt) st.o[dt] = f32x16{};
  st.negm = f32x16{}; st.mref = 0.f; st.l = 0.f;
  k1_phase_b(c, st, q, kb_, vb_, std::make_integer_sequence<int, KV_TILES>{});

  const float lt = half_sum(st.l);
  const float inv = 1.0f / lt;
  if (xrow < SQ) {
    const int g = b * SQ + xrow;
    const int head = kvh * 2 + hh;
    u32x4* dst = ofrag + ((size_t)((p * 28 + (g >> 5)) * 256 + head * 8)) * 64 + h * 32 + (g & 31);
#pragma unroll
    for (int dt = 0; dt < 4; ++dt) {
#pragma unroll
      for (int i = 0; i < 16; ++i) st.o[dt][i] *= inv;
      dst[(2 * dt) * 64] = __builtin_bit_cast(u32x4, pack8(st.o[dt], 0));
      dst[(2 * dt + 1) * 64] = __builtin_bit_cast(u32x4, pack8(st.o[dt], 1));
    }
  }
}

constexpr int K2_D = 5, K2_STEPS = 32, K2_LDS = 163840;

struct K2Ctx {
  __amdgpu_buffer_rsrc_t so, sw;
  unsigned ring, va, vb, soa, sob;
};
template <int I> DI void k2_issue(const K2Ctx& c) {
  if constexpr (I < K2_STEPS) {
    const unsigned sb = c.ring + (I % K2_D) * 4096;
    dma16<0>(sb, c.va, c.so, c.soa + I * 1024);
    dma16<0>(sb + 1024, c.va, c.so, c.soa + I * 1024 + 256 * 1024);
    dma16<0>(sb + 2048, c.vb, c.sw, c.sob + I * 16 * DM * 2);
    dma16<0>(sb + 3072, c.vb, c.sw, c.sob + (I * 16 + 8) * DM * 2);
  }
}
constexpr int k2_wait(int i) { return 4 * ((K2_STEPS - 1 - i) < (K2_D - 1) ? (K2_STEPS - 1 - i) : (K2_D - 1)); }

template <int I>
DI void k2_step(const K2Ctx& c, f32x16 (&acc)[2][2], lds_cptr ab, lds_cptr (&bb)[2]) {
  wait_vm<k2_wait(I)>();
  constexpr int so = (I % K2_D) * 4096;
  bf16x8 of[2], wf[2];
  of[0] = lds_rd128(ab + so);
  of[1] = lds_rd128(ab + so + 1024);
#pragma unroll
  for (int nb = 0; nb < 2; ++nb) wf[nb] = cat8(lds_rdtr(bb[nb] + so + 2048), lds_rdtr(bb[nb] + so + 3072));
  wait_lgkm0();
  SBAR();
  k2_issue<I + K2_D>(c);
#pragma unroll
  for (int nb = 0; nb < 2; ++nb)
#pragma unroll
    for (int mb = 0; mb < 2; ++mb) acc[nb][mb] = MFMA(wf[nb], of[mb], acc[nb][mb]);
}
template <int... Is>
DI void k2_loop(const K2Ctx& c, f32x16 (&acc)[2][2], lds_cptr ab, lds_cptr (&bb)[2], std::integer_sequence<int, Is...>) {
  (k2_step<Is>(c, acc, ab, bb), ...);
}

__global__ __launch_bounds__(512, 2) void k2_oproj(const u32x4* __restrict__ ofrag, const __hip_bfloat16* __restrict__ woA,
                                                   const __hip_bfloat16* __restrict__ woB, __hip_bfloat16* __restrict__ out) {
  extern __shared__ __attribute__((aligned(16))) char lds[];
  const int tid = threadIdx.x, lane = tid & 63;
  const unsigned wid = __builtin_amdgcn_readfirstlane(tid >> 6);
  const int bid = blockIdx.x, xcd = bid & 7, idx = bid >> 3;
  const int p = xcd >> 2, q4 = xcd & 3;
  const int t0 = (126 * q4) >> 2, t1 = (126 * (q4 + 1)) >> 2;
  const int tile = t0 + idx;
  if (tile >= t1) return;
  const int mt = tile / 9, nt = tile - mt * 9;
  const int r = lane & 31, h = lane >> 5, blk = (lane >> 4) & 1, qq = (lane & 15) >> 2, pp = lane & 3;

  K2Ctx c;
  c.ring = (unsigned)(uintptr_t)lds + wid * (K2_D * 4096);
  c.so = make_srd(ofrag + (size_t)((p * 28 + 2 * mt) * 256) * 64, 2 * 256 * 1024);
  c.sw = make_srd((p ? woB : woA) + nt * 64, (NQ * DM - nt * 64) * 2);
  c.va = lane * 16;
  {
    const unsigned kr = lane >> 3;
    c.vb = kr * (DM * 2) + (((lane & 7) ^ (((kr >> 1) & 1) << 2)) << 4);
  }
  c.soa = wid * K2_STEPS * 1024;
  c.sob = wid * K2_STEPS * 16 * DM * 2;
  k2_issue<0>(c); k2_issue<1>(c); k2_issue<2>(c); k2_issue<3>(c); k2_issue<4>(c);

  const lds_cptr R = (lds_cptr)lds + wid * (K2_D * 4096);
  lds_cptr ab = R + lane * 16;
  lds_cptr bb[2];
#pragma unroll
  for (int nb = 0; nb < 2; ++nb) bb[nb] = R + (4 * h + qq) * 128 + ((nb ^ ((qq >> 1) & 1)) << 6) + blk * 32 + pp * 8;
  f32x16 acc[2][2] = {};
  k2_loop(c, acc, ab, bb, std::make_integer_sequence<int, K2_STEPS>{});

  __syncthreads();
  f32x4* red = (f32x4*)lds;
#pragma unroll
  for (int nb = 0; nb < 2; ++nb)
#pragma unroll
    for (int mb = 0; mb < 2; ++mb)
#pragma unroll
      for (int g = 0; g < 4; ++g) {
        const f32x4 v = {acc[nb][mb][4 * g], acc[nb][mb][4 * g + 1], acc[nb][mb][4 * g + 2], acc[nb][mb][4 * g + 3]};
        red[(wid * 16 + (nb * 2 + mb) * 4 + g) * 64 + lane] = v;
      }
  __syncthreads();
#pragma unroll
  for (int j = 0; j < 2; ++j) {
    const int Q = wid * 2 + j;
    f32x4 s = red[Q * 64 + lane];
#pragma unroll
    for (int w = 1; w < 8; ++w) s += red[(w * 16 + Q) * 64 + lane];
    const int nb = Q >> 3, mb = (Q >> 2) & 1, g = Q & 3;
    const int row = mt * 64 + mb * 32 + r;
    const int n = nt * 64 + nb * 32 + 8 * g + 4 * h;
    u32x2 o = {cvtpk(s[0], s[1]), cvtpk(s[2], s[3])};
    *(u32x2*)(out + (size_t)row * (2 * DM) + p * DM + n) = o;
  }
}

extern "C" void kernel_launch(void* const* d_in, const int* in_sizes, int n_in,
                              void* d_out, int out_size, void* d_ws, size_t ws_size,
                              hipStream_t stream) {
  const __hip_bfloat16* xA = (const __hip_bfloat16*)d_in[0];
  const __hip_bfloat16* kA = (const __hip_bfloat16*)d_in[1];
  const __hip_bfloat16* vA = (const __hip_bfloat16*)d_in[2];
  const __hip_bfloat16* wqA = (const __hip_bfloat16*)d_in[3];
  const __hip_bfloat16* woA = (const __hip_bfloat16*)d_in[4];
  const __hip_bfloat16* xB = (const __hip_bfloat16*)d_in[5];
  const __hip_bfloat16* kB = (const __hip_bfloat16*)d_in[6];
  const __hip_bfloat16* vB = (const __hip_bfloat16*)d_in[7];
  const __hip_bfloat16* wqB = (const __hip_bfloat16*)d_in[8];
  const __hip_bfloat16* woB = (const __hip_bfloat16*)d_in[9];
  u32x4* ofrag = (u32x4*)d_ws;
  hipFuncSetAttribute((const void*)k1_attn, hipFuncAttributeMaxDynamicSharedMemorySize, K1_LDS);
  hipFuncSetAttribute((const void*)k2_oproj, hipFuncAttributeMaxDynamicSharedMemorySize, K2_LDS);
  hipLaunchKernelGGL(k1_attn, dim3(256), dim3(512), K1_LDS, stream, xA, kA, vA, wqA, xB, kB, vB, wqB, ofrag);
  hipLaunchKernelGGL(k2_oproj, dim3(256), dim3(512), K2_LDS, stream, (const u32x4*)ofrag, woA, woB, (__hip_bfloat16*)d_out);
}
```

```cpp
#include <hip/hip_runtime.h>
#include <hip/hip_bf16.h>
#include <stdint.h>
#include <utility>

typedef short bf16x8 __attribute__((ext_vector_type(8)));
typedef short s16x4 __attribute__((ext_vector_type(4)));
typedef float f32x16 __attribute__((ext_vector_type(16)));
typedef float f32x4 __attribute__((ext_vector_type(4)));
typedef float f32x2 __attribute__((ext_vector_type(2)));
typedef __bf16 bf16x2_t __attribute__((ext_vector_type(2)));
typedef unsigned u32x4 __attribute__((ext_vector_type(4)));
typedef unsigned u32x2 __attribute__((ext_vector_type(2)));
typedef __attribute__((address_space(3))) const char* lds_cptr;
typedef __attribute__((address_space(3))) char* lds_ptr;
typedef short v4i16_t __attribute__((ext_vector_type(4)));

#define DI __device__ __forceinline__
#define SBAR() __builtin_amdgcn_sched_barrier(0)
#define MFMA(a, b, c) __builtin_amdgcn_mfma_f32_32x32x16_bf16((a), (b), (c), 0, 0, 0)

constexpr int NB = 8, SQ = 112, DM = 576, HQ = 32, HKV = 16, SK = 512, DH = 128;
constexpr int NQ = HQ * DH;
constexpr float QSCALE = 0.088388347648318447f * 1.4426950408889634f;
constexpr float THR = 8.0f;

DI unsigned cvtpk(float lo, float hi) {
  f32x2 v = {lo, hi};
  bf16x2_t b = __builtin_convertvector(v, bf16x2_t);
  return __builtin_bit_cast(unsigned, b);
}
DI bf16x8 pack8(const f32x16& x, int s) {
  u32x4 w = {cvtpk(x[8 * s + 0], x[8 * s + 1]), cvtpk(x[8 * s + 2], x[8 * s + 3]),
             cvtpk(x[8 * s + 4], x[8 * s + 5]), cvtpk(x[8 * s + 6], x[8 * s + 7])};
  return __builtin_bit_cast(bf16x8, w);
}
DI bf16x8 lds_rd128(lds_cptr p) { return *(const __attribute__((address_space(3))) bf16x8*)p; }
DI s16x4 lds_rdtr(lds_cptr p) {
  return __builtin_bit_cast(s16x4, __builtin_amdgcn_ds_read_tr16_b64_v4i16((__attribute__((address_space(3))) v4i16_t*)p));
}
DI bf16x8 cat8(s16x4 lo, s16x4 hi) { return __builtin_shufflevector(lo, hi, 0, 1, 2, 3, 4, 5, 6, 7); }

template <int OFF, int NT = 1>
DI void dma16(unsigned m0v, unsigned voff, __amdgpu_buffer_rsrc_t srd, unsigned soff) {
  unsigned keep;
  if constexpr (NT)
    asm volatile("s_mov_b32 %0, m0\n\ts_mov_b32 m0, %1\n\ts_nop 0\n\t"
                 "buffer_load_dwordx4 %2, %3, %4 offen offset:%5 nt lds\n\ts_mov_b32 m0, %0"
                 : "=&s"(keep) : "s"(m0v), "v"(voff), "s"(srd), "s"(soff), "i"(OFF) : "memory");
  else
    asm volatile("s_mov_b32 %0, m0\n\ts_mov_b32 m0, %1\n\ts_nop 0\n\t"
                 "buffer_load_dwordx4 %2, %3, %4 offen offset:%5 lds\n\ts_mov_b32 m0, %0"
                 : "=&s"(keep) : "s"(m0v), "v"(voff), "s"(srd), "s"(soff), "i"(OFF) : "memory");
}
template <int N> DI void wait_vm_bar() { asm volatile("s_waitcnt vmcnt(%0) lgkmcnt(0)\n\ts_barrier" :: "i"(N) : "memory"); }
template <int N> DI void wait_vm() { asm volatile("s_waitcnt vmcnt(%0)" :: "i"(N) : "memory"); }
DI void wait_lgkm0() { asm volatile("s_waitcnt lgkmcnt(0)" ::: "memory"); }

#define STAMP(c, i)
#define STAMP_MEMBER
#define STAMP_INIT(c)
#define STAMP_OUT(c, base)

#define WARM_LOAD(r, a) asm volatile("global_load_dword %0, %1, off nt" : "+v"(r) : "v"(a) : "memory")
DI __amdgpu_buffer_rsrc_t make_srd(const void* base, unsigned bytes) {
  return __builtin_amdgcn_make_buffer_rsrc((void*)base, (short)0, (int)bytes, 0x00020000);
}

#define A_NT 0
constexpr int A_STAGES = 18;
constexpr int A_SLOT = 24576, A_BASE = 65536, A_X = 0, A_W = 8192;
constexpr int KV_TILES = 8;
constexpr int K1_LDS = 163840;
constexpr int k_off(int t) { constexpr int h[8] = {0, 2, 8, 9, 5, 7, 0, 8}; return h[t] * 16384; }
constexpr int v_off(int t) { constexpr int h[8] = {1, 3, 4, 6, 2, 1, 9, 3}; return h[t] * 16384; }
constexpr int a_wait(int s) { return 6 * ((A_STAGES - 1 - s) < (s == 0 ? 3 : 2) ? (A_STAGES - 1 - s) : (s == 0 ? 3 : 2)); }
constexpr int b_wait(int j) { return j <= 4 ? 20 : j == 5 ? 16 : j == 6 ? 8 : j == 7 ? 4 : 0; }

struct K1Ctx {
  __amdgpu_buffer_rsrc_t sx, sw, sk, sv;
  unsigned ldsb, wid, w4;
  unsigned warm;
  unsigned v0, v1, v2;
  STAMP_MEMBER
};

template <int S> DI void k1_issue_a(const K1Ctx& c) {
  if constexpr (S < A_STAGES) {
    const unsigned sb = c.ldsb + A_BASE + (S % 4) * A_SLOT;
    dma16<0, A_NT>(sb + A_X + c.w4 * 2048, c.v0, c.sx, S * 64);
    dma16<0, A_NT>(sb + A_X + c.w4 * 2048 + 1024, c.v0, c.sx, S * 64 + 16 * DM * 2);
    dma16<0, A_NT>(sb + A_W + c.w4 * 4096, c.v1, c.sw, S * 32 * 8192);
    dma16<0, A_NT>(sb + A_W + c.w4 * 4096 + 1024, c.v2, c.sw, S * 32 * 8192);
    dma16<0, A_NT>(sb + A_W + c.w4 * 4096 + 2048, c.v1, c.sw, S * 32 * 8192 + 4 * 8192);
    dma16<0, A_NT>(sb + A_W + c.w4 * 4096 + 3072, c.v2, c.sw, S * 32 * 8192 + 4 * 8192);
  }
}
template <int T, int P> DI void k1_issue_k_piece(const K1Ctx& c) {
  const unsigned sb = c.ldsb + k_off(T) + (P >> 1) * 8192 + c.w4 * 2048 + (P & 1) * 1024;
  dma16<0>(sb, (P & 1) ? c.v1 : c.v0, c.sk, T * 16384 + (P >> 1) * 128);
}
template <int T, int P> DI void k1_issue_v_piece(const K1Ctx& c) {
  dma16<P * 1024>(c.ldsb + v_off(T) + c.w4 * 4096, c.v2, c.sv, T * 16384);
}
template <int T> DI void k1_issue_k(const K1Ctx& c) {
  k1_issue_k_piece<T, 0>(c); k1_issue_k_piece<T, 1>(c); k1_issue_k_piece<T, 2>(c); k1_issue_k_piece<T, 3>(c);
}
template <int T> DI void k1_issue_v(const K1Ctx& c) {
  k1_issue_v_piece<T, 0>(c); k1_issue_v_piece<T, 1>(c); k1_issue_v_piece<T, 2>(c); k1_issue_v_piece<T, 3>(c);
}
template <int J> DI void k1_issue_b(const K1Ctx& c) {
  if constexpr (J == 0) { k1_issue_v<2>(c); k1_issue_k<4>(c); }
  if constexpr (J == 1) { k1_issue_v<3>(c); k1_issue_k<5>(c); }
  if constexpr (J == 2) { k1_issue_k<6>(c); k1_issue_v<4>(c); }
  if constexpr (J == 3) { k1_issue_k<7>(c); k1_issue_v<5>(c); }
  if constexpr (J == 4) { k1_issue_v<6>(c); k1_issue_v<7>(c); }
}
DI void bar_lgkm() { asm volatile("s_waitcnt lgkmcnt(0)\n\ts_barrier" ::: "memory"); }
#define PIN(x) asm volatile("" : "+v"(x))

struct AFrag { bf16x8 x; s16x4 lo[4], hi[4]; };
template <int S, int KS> DI void k1_read_a(AFrag& f, lds_cptr (&xb)[2], lds_cptr (&wb)[4]) {
  constexpr int so = A_BASE + (S % 4) * A_SLOT;
  f.x = lds_rd128(xb[KS] + so + A_X);
#pragma unroll
  for (int dt = 0; dt < 4; ++dt) {
    f.lo[dt] = lds_rdtr(wb[dt] + so + A_W + KS * 8192);
    f.hi[dt] = lds_rdtr(wb[dt] + so + A_W + KS * 8192 + 2048);
  }
}
DI void k1_mma_a(f32x16 (&qa)[4], const AFrag& f) {
#pragma unroll
  for (int dt = 0; dt < 4; ++dt) qa[dt] = MFMA(cat8(f.lo[dt], f.hi[dt]), f.x, qa[dt]);
}
template <int S>
DI void k1_stage_a(const K1Ctx& c, f32x16 (&qa)[4], AFrag& f0, AFrag& f1, lds_cptr (&xb)[2], lds_cptr (&wb)[4]) {
  k1_read_a<S, 1>(f1, xb, wb);
  SBAR();
  k1_mma_a(qa, f0);
  SBAR();
  if constexpr (S + 1 < A_STAGES) {
    if (c.wid < 4) wait_vm<a_wait(S + 1)>();
    bar_lgkm();
    STAMP(c, 2 + S);
    k1_read_a<S + 1, 0>(f0, xb, wb);
    SBAR();
  }
  k1_mma_a(qa, f1);
  SBAR();
  if (c.wid < 4) k1_issue_a<S + 4>(c);
  else {
    if constexpr (S < 4) k1_issue_k_piece<0, S % 4>(c);
    else if constexpr (S < 8) k1_issue_v_piece<0, S % 4>(c);
    else if constexpr (S < 12) k1_issue_k_piece<1, S % 4>(c);
    else if constexpr (S < 15) k1_issue_v_piece<1, S % 4>(c);
    else if constexpr (S == 15) { k1_issue_v_piece<1, 3>(c); k1_issue_k<2>(c); k1_issue_k<3>(c); }
  }
  SBAR();
}
template <int... Is>
DI void k1_phase_a(const K1Ctx& c, f32x16 (&qa)[4], lds_cptr (&xb)[2], lds_cptr (&wb)[4], std::integer_sequence<int, Is...>) {
  AFrag f0, f1;
  if (c.wid < 4) wait_vm<a_wait(0)>();
  bar_lgkm();
  STAMP(c, 1);
  k1_read_a<0, 0>(f0, xb, wb);
  (k1_stage_a<Is>(c, qa, f0, f1, xb, wb), ...);
}

DI float half_max(float x) {
  auto rr = __builtin_amdgcn_permlane32_swap(__float_as_uint(x), __float_as_uint(x), false, false);
  return fmaxf(__uint_as_float(rr[0]), __uint_as_float(rr[1]));
}
DI float half_sum(float x) {
  auto rr = __builtin_amdgcn_permlane32_swap(__float_as_uint(x), __float_as_uint(x), false, false);
  return __uint_as_float(rr[0]) + __uint_as_float(rr[1]);
}
#define MX3(a, b, c) __builtin_fmaxf(__builtin_fmaxf((a), (b)), (c))

struct AttnState {
  f32x16 o[4];
  f32x16 negm;
  f32x16 s[2][2];
  u32x4 pk[4];
  bf16x8 kf[4];
  s16x4 vlo[4], vhi[4];
  float mref, l;
};

template <int T, int M> DI void k1_kread(AttnState& st, lds_cptr (&kb_)[4]) {
  st.kf[M & 3] = lds_rd128(kb_[M & 3] + k_off(T) + ((M & 7) >> 2) * 8192 + (M >> 3) * 4096);
}
template <int T, int M> DI void k1_vread(AttnState& st, lds_cptr (&vb_)[4]) {
  st.vlo[M & 3] = lds_rdtr(vb_[M & 3] + v_off(T) + (M >> 2) * 4096);
  st.vhi[M & 3] = lds_rdtr(vb_[M & 3] + v_off(T) + (M >> 2) * 4096 + 2048);
}
template <int J> DI void k1_bar_b(const K1Ctx& c) {
  if (c.wid >= 4) wait_vm<b_wait(J)>();
  if constexpr (J == 0) asm volatile("" :: "v"(c.warm));
  bar_lgkm();
  STAMP(c, 20 + J);
}

template <int T, int M>
DI void k1_gap_qk(AttnState& st, const bf16x8 (&q)[8], lds_cptr (&kb_)[4], lds_cptr (&vb_)[4], float& sacc, float& rm) {
  constexpr int X = T & 1, Y = X ^ 1, kb = M >> 3, ds = M & 7;
  const bf16x8 kf = st.kf[M & 3];
  if constexpr (ds == 0) {
    if constexpr (T == 0) { f32x16 z = {}; st.s[X][kb] = MFMA(kf, q[ds], z); }
    else st.s[X][kb] = MFMA(kf, q[ds], st.negm);
  } else st.s[X][kb] = MFMA(kf, q[ds], st.s[X][kb]);
  if constexpr (M + 4 < 16) k1_kread<T, M + 4>(st, kb_);
  if constexpr (T > 0) {
    if constexpr (M >= 12) k1_vread<T - 1, M - 12>(st, vb_);
    const f32x16& P = st.s[Y][M >> 3];
    constexpr int e = (M & 7) * 2;
    sacc += P[e]; sacc += P[e + 1];
    st.pk[M >> 2][M & 3] = cvtpk(P[e], P[e + 1]);
    PIN(sacc); PIN(st.pk[M >> 2]);
  }
  if constexpr (M >= 10 && M < 14) {
    constexpr int e = (M - 10) * 4;
    const f32x16& S0 = st.s[X][0];
    if constexpr (M == 10) rm = MX3(S0[0], S0[1], S0[2]); else rm = MX3(rm, S0[e - 1], S0[e]);
    if constexpr (M == 10) rm = fmaxf(rm, S0[3]);
    else rm = MX3(rm, S0[e + 1], S0[e + 2]);
    if constexpr (M == 13) rm = fmaxf(rm, S0[15]);
    PIN(rm);
  }
  SBAR();
}
template <int T, int... Ms>
DI void k1_qk(AttnState& st, const bf16x8 (&q)[8], lds_cptr (&kb_)[4], lds_cptr (&vb_)[4], float& sacc, float& rm,
              std::integer_sequence<int, Ms...>) {
  (k1_gap_qk<T, Ms>(st, q, kb_, vb_, sacc, rm), ...);
}
template <int T, int M, int E0, int E1>
DI void k1_gap_pv(AttnState& st, lds_cptr (&kb_)[4], lds_cptr (&vb_)[4]) {
  constexpr int X = T & 1;
  st.o[M & 3] = MFMA(cat8(st.vlo[M & 3], st.vhi[M & 3]), __builtin_bit_cast(bf16x8, st.pk[M >> 2]), st.o[M & 3]);
  if constexpr (M + 4 < 16) k1_vread<T - 1, M + 4>(st, vb_);
  if constexpr (M >= 12) {
    if constexpr (T + 1 < KV_TILES) k1_kread<T + 1, M - 12>(st, kb_);
    else k1_vread<T, M - 12>(st, vb_);
  }
#pragma unroll
  for (int e = E0; e < E1; ++e) st.s[X][e >> 4][e & 15] = __builtin_amdgcn_exp2f(st.s[X][e >> 4][e & 15]);
  if constexpr (E0 < E1) { PIN(st.s[X][E0 >> 4]); if constexpr (((E1 - 1) >> 4) != (E0 >> 4)) PIN(st.s[X][(E1 - 1) >> 4]); }
  SBAR();
}
constexpr int pv_e(int m) { return m < 2 ? 0 : m >= 16 ? 32 : ((m - 2) * 32 + 6) / 14; }
template <int T, int... Ms>
DI void k1_pv_tail(AttnState& st, lds_cptr (&kb_)[4], lds_cptr (&vb_)[4], std::integer_sequence<int, Ms...>) {
  (k1_gap_pv<T, Ms + 2, pv_e(Ms + 2), pv_e(Ms + 3)>(st, kb_, vb_), ...);
}

template <int T>
DI void k1_step_b(const K1Ctx& c, AttnState& st, const bf16x8 (&q)[8], lds_cptr (&kb_)[4], lds_cptr (&vb_)[4]) {
  constexpr int X = T & 1;
  float sacc = 0.f, rm = 0.f;
  k1_qk<T>(st, q, kb_, vb_, sacc, rm, std::make_integer_sequence<int, 16>{});
  st.l += sacc;
  k1_bar_b<T + 1>(c);
  if constexpr (T == 0) {
    if (c.wid >= 4) k1_issue_b<T + 1>(c);
    SBAR();
    k1_kread<1, 0>(st, kb_); k1_kread<1, 1>(st, kb_); k1_kread<1, 2>(st, kb_); k1_kread<1, 3>(st, kb_);
#pragma unroll
    for (int r = 0; r < 16; r += 2) rm = MX3(rm, st.s[X][1][r], st.s[X][1][r + 1]);
    rm = half_max(rm);
    st.mref = rm;
#pragma unroll
    for (int r = 0; r < 16; ++r) {
      st.s[X][0][r] = __builtin_amdgcn_exp2f(st.s[X][0][r] - rm);
      st.s[X][1][r] = __builtin_amdgcn_exp2f(st.s[X][1][r] - rm);
      st.negm[r] = -rm;
    }
    SBAR();
  } else {
    k1_gap_pv<T, 0, 0, 0>(st, kb_, vb_);
#pragma unroll
    for (int r = 0; r < 16; r += 2) rm = MX3(rm, st.s[X][1][r], st.s[X][1][r + 1]);
    PIN(rm);
    k1_gap_pv<T, 1, 0, 0>(st, kb_, vb_);
    rm = half_max(rm);
    bool resc = false;
    float f = 1.f;
    if (__builtin_expect(__any(rm > THR), 0)) {
      const float dl = fmaxf(rm, 0.f);
      st.mref += dl;
      f = __builtin_amdgcn_exp2f(-dl);
      st.l *= f;
#pragma unroll
      for (int r = 0; r < 16; ++r) { st.s[X][0][r] -= dl; st.s[X][1][r] -= dl; st.negm[r] = -st.mref; }
      resc = true;
    }
    SBAR();
    if (c.wid >= 4) k1_issue_b<T + 1>(c);
    SBAR();
    k1_pv_tail<T>(st, kb_, vb_, std::make_integer_sequence<int, 14>{});
    if (__builtin_expect(resc, 0)) {
#pragma unroll
      for (int dt = 0; dt < 4; ++dt)
#pragma unroll
        for (int r = 0; r < 16; ++r) st.o[dt][r] *= f;
    }
    SBAR();
  }
}
template <int M> DI void k1_gap_last(AttnState& st, lds_cptr (&vb_)[4]) {
  constexpr int T = KV_TILES - 1;
  st.o[M & 3] = MFMA(cat8(st.vlo[M & 3], st.vhi[M & 3]), __builtin_bit_cast(bf16x8, st.pk[M >> 2]), st.o[M & 3]);
  if constexpr (M + 4 < 16) k1_vread<T, M + 4>(st, vb_);
  SBAR();
}
template <int... Ms> DI void k1_last(AttnState& st, lds_cptr (&vb_)[4], std::integer_sequence<int, Ms...>) {
  constexpr int X = (KV_TILES - 1) & 1;
  float sacc = 0.f;
#pragma unroll
  for (int kb = 0; kb < 2; ++kb)
#pragma unroll
    for (int e = 0; e < 16; e += 2) {
      sacc += st.s[X][kb][e]; sacc += st.s[X][kb][e + 1];
      st.pk[kb * 2 + (e >> 3)][(e >> 1) & 3] = cvtpk(st.s[X][kb][e], st.s[X][kb][e + 1]);
    }
  st.l += sacc;
  SBAR();
  (k1_gap_last<Ms>(st, vb_), ...);
}
template <int... Is>
DI void k1_phase_b(const K1Ctx& c, AttnState& st, const bf16x8 (&q)[8], lds_cptr (&kb_)[4], lds_cptr (&vb_)[4],
                   std::integer_sequence<int, Is...>) {
  k1_bar_b<0>(c);
  k1_kread<0, 0>(st, kb_); k1_kread<0, 1>(st, kb_); k1_kread<0, 2>(st, kb_); k1_kread<0, 3>(st, kb_);
  SBAR();
  if (c.wid >= 4) k1_issue_b<0>(c);
  SBAR();
  (k1_step_b<Is>(c, st, q, kb_, vb_), ...);
  k1_last(st, vb_, std::make_integer_sequence<int, 16>{});
}

__global__ __launch_bounds__(512, 2) void k1_attn(const __hip_bfloat16* __restrict__ xA, const __hip_bfloat16* __restrict__ kA,
                                                  const __hip_bfloat16* __restrict__ vA, const __hip_bfloat16* __restrict__ wqA,
                                                  const __hip_bfloat16* __restrict__ xB, const __hip_bfloat16* __restrict__ kB,
                                                  const __hip_bfloat16* __restrict__ vB, const __hip_bfloat16* __restrict__ wqB,
                                                  u32x4* __restrict__ ofrag) {
  extern __shared__ __attribute__((aligned(16))) char lds[];
  const int tid = threadIdx.x, lane = tid & 63;
  const unsigned wid = __builtin_amdgcn_readfirstlane(tid >> 6);
  const int bid = blockIdx.x, xcd = bid & 7, idx = bid >> 3;
  const int p = xcd >> 2, kvh = (xcd & 3) * 4 + (idx & 3), b = idx >> 2;
  const int hh = wid >> 2, rb = wid & 3;
  const int r = lane & 31, h = lane >> 5, blk = (lane >> 4) & 1, qq = (lane & 15) >> 2, pp = lane & 3;

  K1Ctx c;
  STAMP_INIT(c); STAMP(c, 0);
  c.ldsb = (unsigned)(uintptr_t)lds;
  c.wid = wid;
  {
    const __hip_bfloat16* x = p ? xB : xA; const __hip_bfloat16* wq = p ? wqB : wqA;
    const __hip_bfloat16* kc = p ? kB : kA; const __hip_bfloat16* vc = p ? vB : vA;
    c.sx = make_srd(x + (size_t)b * SQ * DM, SQ * DM * 2);
    c.sw = make_srd(wq + kvh * 256, (DM * NQ - kvh * 256) * 2);
    c.sk = make_srd(kc + (size_t)(b * HKV + kvh) * SK * DH, SK * DH * 2);
    c.sv = make_srd(vc + (size_t)(b * HKV + kvh) * SK * DH, SK * DH * 2);
  }
  c.w4 = wid & 3;
  c.warm = 0;
  if (wid < 4) {
    const unsigned xr = 32 * c.w4 + (lane >> 2);
    c.v0 = xr * (DM * 2) + (((lane & 3) ^ ((xr >> 2) & 3)) << 4);
    const unsigned k0 = 8 * c.w4 + (lane >> 5), k1 = k0 + 2;
    c.v1 = k0 * 8192 + (((lane & 31) ^ ((k0 & 3) << 2)) << 4);
    c.v2 = k1 * 8192 + (((lane & 31) ^ ((k1 & 3) << 2)) << 4);
    k1_issue_a<0>(c); k1_issue_a<1>(c); k1_issue_a<2>(c); k1_issue_a<3>(c);
  } else {
    const unsigned kr0 = 16 * c.w4 + (lane >> 3), kr1 = kr0 + 8;
    c.v0 = kr0 * 256 + (((lane & 7) ^ ((kr0 >> 1) & 7)) << 4);
    c.v1 = kr1 * 256 + (((lane & 7) ^ ((kr1 >> 1) & 7)) << 4);
    const unsigned vr = 16 * c.w4 + (lane >> 4);
    c.v2 = vr * 256 + (((lane & 15) ^ ((vr & 3) << 2)) << 4);
    {
      const char* wqg = (const char*)(p ? wqB : wqA) + (xcd & 3) * 2048;
      const char* xg = (const char*)(p ? xB : xA);
      {
        const int krow = c.w4 * 128 + 4 * idx + (lane >> 4);
        const char* a = wqg + (size_t)krow * 8192 + (lane & 15) * 128;
        WARM_LOAD(c.warm, a);
      }
      {
        const int f = c.w4 * 64 + lane;
        const int ff = f < 252 ? f : 251;
        const char* a = xg + (size_t)(idx * 28 + ff % 28) * (DM * 2) + (ff / 28) * 128;
        WARM_LOAD(c.warm, a);
      }
      if (c.w4 == 0 && idx < 16) {
        const int krow = 512 + 4 * idx + (lane >> 4);
        const char* a = wqg + (size_t)krow * 8192 + (lane & 15) * 128;
        WARM_LOAD(c.warm, a);
      }
    }
  }

  const lds_cptr L = (lds_cptr)lds;
  const int xrow = rb * 32 + r;
  lds_cptr xb[2];
  xb[0] = L + xrow * 64 + ((h ^ ((xrow >> 2) & 3)) << 4);
  xb[1] = L + xrow * 64 + (((2 + h) ^ ((xrow >> 2) & 3)) << 4);
  lds_cptr wb[4];
  {
    const int psw = ((pp & 1) << 1) | (pp >> 1);
#pragma unroll
    for (int dt = 0; dt < 4; ++dt) wb[dt] = L + (8 * h + qq) * 512 + hh * 256 + ((dt ^ qq) << 6) + blk * 32 + psw * 8;
  }
  f32x16 qa[4] = {};
  k1_phase_a(c, qa, xb, wb, std::make_integer_sequence<int, A_STAGES>{});

  bf16x8 q[8];
#pragma unroll
  for (int dt = 0; dt < 4; ++dt) {
#pragma unroll
    for (int i = 0; i < 16; ++i) qa[dt][i] *= QSCALE;
    q[2 * dt] = pack8(qa[dt], 0);
    q[2 * dt + 1] = pack8(qa[dt], 1);
  }

  lds_cptr kb_[4], vb_[4];
#pragma unroll
  for (int j = 0; j < 4; ++j) {
    kb_[j] = L + r * 128 + (((2 * j + h) ^ ((r >> 1) & 7)) << 4);
    vb_[j] = L + (4 * h + qq) * 256 + ((j ^ qq) << 6) + blk * 32 + pp * 8;
  }
  AttnState st;
#pragma unroll
  for (int dt = 0; dt < 4; ++dt) st.o[dt] = f32x16{};
  st.negm = f32x16{}; st.mref = 0.f; st.l = 0.f;
  k1_phase_b(c, st, q, kb_, vb_, std::make_integer_sequence<int, KV_TILES>{});

  STAMP(c, 30);
  const float lt = half_sum(st.l);
  const float inv = 1.0f / lt;
  if (xrow < SQ) {
    const int g = b * SQ + xrow;
    const int head = kvh * 2 + hh;
    u32x4* dst = ofrag + ((size_t)((p * 28 + (g >> 5)) * 256 + head * 8)) * 64 + h * 32 + (g & 31);
#pragma unroll
    for (int dt = 0; dt < 4; ++dt) {
#pragma unroll
      for (int i = 0; i < 16; ++i) st.o[dt][i] *= inv;
      dst[(2 * dt) * 64] = __builtin_bit_cast(u32x4, pack8(st.o[dt], 0));
      dst[(2 * dt + 1) * 64] = __builtin_bit_cast(u32x4, pack8(st.o[dt], 1));
    }
  }
  STAMP(c, 31);
  STAMP_OUT(c, ofrag + (32u << 20) / 16);
}

#define K2_O_NT 0
#define K2_W_NT 0
#define K2_DEPTH 5
#define K2_IL 0
constexpr int K2_D = K2_DEPTH, K2_STEPS = 32, K2_LDS = 163840;
constexpr int K2_KS = K2_IL ? 8 : 1;

struct K2Ctx {
  __amdgpu_buffer_rsrc_t so, sw;
  unsigned ring, va, vb, soa, sob;
  STAMP_MEMBER
};
template <int I> DI void k2_issue(const K2Ctx& c) {
  if constexpr (I < K2_STEPS) {
    const unsigned sb = c.ring + (I % K2_D) * 4096;
    dma16<0, K2_O_NT>(sb, c.va, c.so, c.soa + I * K2_KS * 1024);
    dma16<0, K2_O_NT>(sb + 1024, c.va, c.so, c.soa + I * K2_KS * 1024 + 256 * 1024);
    dma16<0, K2_W_NT>(sb + 2048, c.vb, c.sw, c.sob + I * K2_KS * 16 * DM * 2);
    dma16<0, K2_W_NT>(sb + 3072, c.vb, c.sw, c.sob + (I * K2_KS * 16 + 8) * DM * 2);
  }
}
template <int... Is> DI void k2_prologue(const K2Ctx& c, std::integer_sequence<int, Is...>) { (k2_issue<Is>(c), ...); }
constexpr int k2_wait(int i) { return 4 * ((K2_STEPS - 1 - i) < (K2_D - 1) ? (K2_STEPS - 1 - i) : (K2_D - 1)); }

template <int I>
DI void k2_step(const K2Ctx& c, f32x16 (&acc)[2][2], lds_cptr ab, lds_cptr (&bb)[2]) {
  wait_vm<k2_wait(I)>();
  STAMP(c, 1 + I);
  constexpr int so = (I % K2_D) * 4096;
  bf16x8 of[2], wf[2];
  of[0] = lds_rd128(ab + so);
  of[1] = lds_rd128(ab + so + 1024);
#pragma unroll
  for (int nb = 0; nb < 2; ++nb) wf[nb] = cat8(lds_rdtr(bb[nb] + so + 2048), lds_rdtr(bb[nb] + so + 3072));
  wait_lgkm0();
  SBAR();
  k2_issue<I + K2_D>(c);
#pragma unroll
  for (int nb = 0; nb < 2; ++nb)
#pragma unroll
    for (int mb = 0; mb < 2; ++mb) acc[nb][mb] = MFMA(wf[nb], of[mb], acc[nb][mb]);
}
template <int... Is>
DI void k2_loop(const K2Ctx& c, f32x16 (&acc)[2][2], lds_cptr ab, lds_cptr (&bb)[2], std::integer_sequence<int, Is...>) {
  (k2_step<Is>(c, acc, ab, bb), ...);
}

__global__ __launch_bounds__(512, 2) void k2_oproj(const u32x4* __restrict__ ofrag, const __hip_bfloat16* __restrict__ woA,
                                                   const __hip_bfloat16* __restrict__ woB, __hip_bfloat16* __restrict__ out) {
  extern __shared__ __attribute__((aligned(16))) char lds[];
  const int tid = threadIdx.x, lane = tid & 63;
  const unsigned wid = __builtin_amdgcn_readfirstlane(tid >> 6);
  const int bid = blockIdx.x, xcd = bid & 7, idx = bid >> 3;
  const int p = xcd >> 2, q4 = xcd & 3;
  const int t0 = (126 * q4) >> 2, t1 = (126 * (q4 + 1)) >> 2;
  const int tile = t0 + idx;
  if (tile >= t1) return;
  const int mt = tile / 9, nt = tile - mt * 9;
  const int r = lane & 31, h = lane >> 5, blk = (lane >> 4) & 1, qq = (lane & 15) >> 2, pp = lane & 3;

  K2Ctx c;
  STAMP_INIT(c); STAMP(c, 0);
  c.ring = (unsigned)(uintptr_t)lds + wid * (K2_D * 4096);
  c.so = make_srd(ofrag + (size_t)((p * 28 + 2 * mt) * 256) * 64, 2 * 256 * 1024);
  c.sw = make_srd((p ? woB : woA) + nt * 64, (NQ * DM - nt * 64) * 2);
  c.va = lane * 16;
  {
    const unsigned kr = lane >> 3;
    c.vb = kr * (DM * 2) + (((lane & 7) ^ (((kr >> 1) & 1) << 2)) << 4);
  }
  c.soa = wid * (K2_IL ? 1 : K2_STEPS) * 1024;
  c.sob = wid * (K2_IL ? 1 : K2_STEPS) * 16 * DM * 2;
  k2_prologue(c, std::make_integer_sequence<int, K2_D>{});

  const lds_cptr R = (lds_cptr)lds + wid * (K2_D * 4096);
  lds_cptr ab = R + lane * 16;
  lds_cptr bb[2];
#pragma unroll
  for (int nb = 0; nb < 2; ++nb) bb[nb] = R + (4 * h + qq) * 128 + ((nb ^ ((qq >> 1) & 1)) << 6) + blk * 32 + pp * 8;
  f32x16 acc[2][2] = {};
  k2_loop(c, acc, ab, bb, std::make_integer_sequence<int, K2_STEPS>{});

  STAMP(c, 33);
  __syncthreads();
  f32x4* red = (f32x4*)lds;
#pragma unroll
  for (int nb = 0; nb < 2; ++nb)
#pragma unroll
    for (int mb = 0; mb < 2; ++mb)
#pragma unroll
      for (int g = 0; g < 4; ++g) {
        const f32x4 v = {acc[nb][mb][4 * g], acc[nb][mb][4 * g + 1], acc[nb][mb][4 * g + 2], acc[nb][mb][4 * g + 3]};
        red[(wid * 16 + (nb * 2 + mb) * 4 + g) * 64 + lane] = v;
      }
  __syncthreads();
#pragma unroll
  for (int j = 0; j < 2; ++j) {
    const int Q = wid * 2 + j;
    f32x4 s = red[Q * 64 + lane];
#pragma unroll
    for (int w = 1; w < 8; ++w) s += red[(w * 16 + Q) * 64 + lane];
    const int nb = Q >> 3, mb = (Q >> 2) & 1, g = Q & 3;
    const int row = mt * 64 + mb * 32 + r;
    const int n = nt * 64 + nb * 32 + 8 * g + 4 * h;
    u32x2 o = {cvtpk(s[0], s[1]), cvtpk(s[2], s[3])};
    *(u32x2*)(out + (size_t)row * (2 * DM) + p * DM + n) = o;
  }
  STAMP(c, 34);
  STAMP_OUT(c, (u32x4*)ofrag + (48u << 20) / 16);
}

extern "C" void kernel_launch(void* const* d_in, const int* in_sizes, int n_in,
                              void* d_out, int out_size, void* d_ws, size_t ws_size,
                              hipStream_t stream) {
  const __hip_bfloat16* xA = (const __hip_bfloat16*)d_in[0];
  const __hip_bfloat16* kA = (const __hip_bfloat16*)d_in[1];
  const __hip_bfloat16* vA = (const __hip_bfloat16*)d_in[2];
  const __hip_bfloat16* wqA = (const __hip_bfloat16*)d_in[3];
  const __hip_bfloat16* woA = (const __hip_bfloat16*)d_in[4];
  const __hip_bfloat16* xB = (const __hip_bfloat16*)d_in[5];
  const __hip_bfloat16* kB = (const __hip_bfloat16*)d_in[6];
  const __hip_bfloat16* vB = (const __hip_bfloat16*)d_in[7];
  const __hip_bfloat16* wqB = (const __hip_bfloat16*)d_in[8];
  const __hip_bfloat16* woB = (const __hip_bfloat16*)d_in[9];
  u32x4* ofrag = (u32x4*)d_ws;
  hipFuncSetAttribute((const void*)k1_attn, hipFuncAttributeMaxDynamicSharedMemorySize, K1_LDS);
  hipFuncSetAttribute((const void*)k2_oproj, hipFuncAttributeMaxDynamicSharedMemorySize, K2_LDS);
  hipLaunchKernelGGL(k1_attn, dim3(256), dim3(512), K1_LDS, stream, xA, kA, vA, wqA, xB, kB, vB, wqB, ofrag);
  hipLaunchKernelGGL(k2_oproj, dim3(256), dim3(512), K2_LDS, stream, (const u32x4*)ofrag, woA, woB, (__hip_bfloat16*)d_out);
}
```

```cpp
#include <hip/hip_runtime.h>
#include <hip/hip_bf16.h>
#include <stdint.h>
#include <utility>

typedef short bf16x8 __attribute__((ext_vector_type(8)));
typedef short s16x4 __attribute__((ext_vector_type(4)));
typedef float f32x16 __attribute__((ext_vector_type(16)));
typedef float f32x4 __attribute__((ext_vector_type(4)));
typedef float f32x2 __attribute__((ext_vector_type(2)));
typedef __bf16 bf16x2_t __attribute__((ext_vector_type(2)));
typedef unsigned u32x4 __attribute__((ext_vector_type(4)));
typedef unsigned u32x2 __attribute__((ext_vector_type(2)));
typedef __attribute__((address_space(3))) const char* lds_cptr;
typedef __attribute__((address_space(3))) char* lds_ptr;
typedef short v4i16_t __attribute__((ext_vector_type(4)));

#define DI __device__ __forceinline__
#define SBAR() __builtin_amdgcn_sched_barrier(0)
#define MFMA(a, b, c) __builtin_amdgcn_mfma_f32_32x32x16_bf16((a), (b), (c), 0, 0, 0)

constexpr int NB = 8, SQ = 112, DM = 576, HQ = 32, HKV = 16, SK = 512, DH = 128;
constexpr int NQ = HQ * DH;
constexpr float QSCALE = 0.088388347648318447f * 1.4426950408889634f;
constexpr float THR = 8.0f;

DI unsigned cvtpk(float lo, float hi) {
  f32x2 v = {lo, hi};
  bf16x2_t b = __builtin_convertvector(v, bf16x2_t);
  return __builtin_bit_cast(unsigned, b);
}
DI bf16x8 pack8(const f32x16& x, int s) {
  u32x4 w = {cvtpk(x[8 * s + 0], x[8 * s + 1]), cvtpk(x[8 * s + 2], x[8 * s + 3]),
             cvtpk(x[8 * s + 4], x[8 * s + 5]), cvtpk(x[8 * s + 6], x[8 * s + 7])};
  return __builtin_bit_cast(bf16x8, w);
}
DI bf16x8 lds_rd128(lds_cptr p) { return *(const __attribute__((address_space(3))) bf16x8*)p; }
DI s16x4 lds_rdtr(lds_cptr p) {
  return __builtin_bit_cast(s16x4, __builtin_amdgcn_ds_read_tr16_b64_v4i16((__attribute__((address_space(3))) v4i16_t*)p));
}
DI bf16x8 cat8(s16x4 lo, s16x4 hi) { return __builtin_shufflevector(lo, hi, 0, 1, 2, 3, 4, 5, 6, 7); }

template <int OFF, int NT = 1>
DI void dma16(unsigned m0v, unsigned voff, __amdgpu_buffer_rsrc_t srd, unsigned soff) {
  unsigned keep;
  if constexpr (NT)
    asm volatile("s_mov_b32 %0, m0\n\ts_mov_b32 m0, %1\n\ts_nop 0\n\t"
                 "buffer_load_dwordx4 %2, %3, %4 offen offset:%5 nt lds\n\ts_mov_b32 m0, %0"
                 : "=&s"(keep) : "s"(m0v), "v"(voff), "s"(srd), "s"(soff), "i"(OFF) : "memory");
  else
    asm volatile("s_mov_b32 %0, m0\n\ts_mov_b32 m0, %1\n\ts_nop 0\n\t"
                 "buffer_load_dwordx4 %2, %3, %4 offen offset:%5 lds\n\ts_mov_b32 m0, %0"
                 : "=&s"(keep) : "s"(m0v), "v"(voff), "s"(srd), "s"(soff), "i"(OFF) : "memory");
}
template <int N> DI void wait_vm_bar() { asm volatile("s_waitcnt vmcnt(%0) lgkmcnt(0)\n\ts_barrier" :: "i"(N) : "memory"); }
template <int N> DI void wait_vm() { asm volatile("s_waitcnt vmcnt(%0)" :: "i"(N) : "memory"); }
DI void wait_lgkm0() { asm volatile("s_waitcnt lgkmcnt(0)" ::: "memory"); }

#define STAMP(c, i)
#define STAMP_MEMBER
#define STAMP_INIT(c)
#define STAMP_OUT(c, base)

#define WARM_LOAD(r, a) asm volatile("global_load_dword %0, %1, off nt" : "+v"(r) : "v"(a) : "memory")
DI __amdgpu_buffer_rsrc_t make_srd(const void* base, unsigned bytes) {
  return __builtin_amdgcn_make_buffer_rsrc((void*)base, (short)0, (int)bytes, 0x00020000);
}

#define KV_NT 1
#define A_NT 0
constexpr int A_STAGES = 18;
constexpr int A_SLOT = 24576, A_BASE = 65536, A_X = 0, A_W = 8192;
constexpr int KV_TILES = 8;
constexpr int K1_LDS = 163840;
constexpr int k_off(int t) { constexpr int h[8] = {0, 2, 8, 9, 5, 7, 0, 8}; return h[t] * 16384; }
constexpr int v_off(int t) { constexpr int h[8] = {1, 3, 4, 6, 2, 1, 9, 3}; return h[t] * 16384; }
constexpr int a_wait(int s) { return 6 * ((A_STAGES - 1 - s) < (s == 0 ? 3 : 2) ? (A_STAGES - 1 - s) : (s == 0 ? 3 : 2)); }
constexpr int b_wait(int j) { constexpr int n[9] = {16, 13, 14, 14, 14, 10, 9, 4, 0}; return n[j]; }

struct K1Ctx {
  __amdgpu_buffer_rsrc_t sx, sw, sk, sv;
  unsigned ldsb, wid, w4, grp, vt;
  mutable unsigned warm;
  unsigned v0, v1, v2;
  STAMP_MEMBER
};

template <int S> DI void k1_issue_a(const K1Ctx& c) {
  if constexpr (S < A_STAGES) {
    const unsigned sb = c.ldsb + A_BASE + (S % 4) * A_SLOT;
    dma16<0, A_NT>(sb + A_X + c.w4 * 2048, c.v0, c.sx, S * 64);
    dma16<0, A_NT>(sb + A_X + c.w4 * 2048 + 1024, c.v0, c.sx, S * 64 + 16 * DM * 2);
    dma16<0, A_NT>(sb + A_W + c.w4 * 4096, c.v1, c.sw, S * 32 * 8192);
    dma16<0, A_NT>(sb + A_W + c.w4 * 4096 + 1024, c.v2, c.sw, S * 32 * 8192);
    dma16<0, A_NT>(sb + A_W + c.w4 * 4096 + 2048, c.v1, c.sw, S * 32 * 8192 + 4 * 8192);
    dma16<0, A_NT>(sb + A_W + c.w4 * 4096 + 3072, c.v2, c.sw, S * 32 * 8192 + 4 * 8192);
  }
}
template <int T, int P> DI void k1_issue_k_piece(const K1Ctx& c) {
  const unsigned sb = c.ldsb + k_off(T) + (P >> 1) * 8192 + c.w4 * 2048 + (P & 1) * 1024;
  dma16<0, KV_NT>(sb, (P & 1) ? c.v1 : c.v0, c.sk, T * 16384 + (P >> 1) * 128);
}
template <int T, int P> DI void k1_issue_v_piece(const K1Ctx& c) {
  dma16<P * 1024, KV_NT>(c.ldsb + v_off(T) + c.w4 * 4096, c.v2, c.sv, T * 16384);
}
template <int T> DI void k1_issue_k(const K1Ctx& c) {
  k1_issue_k_piece<T, 0>(c); k1_issue_k_piece<T, 1>(c); k1_issue_k_piece<T, 2>(c); k1_issue_k_piece<T, 3>(c);
}
template <int T> DI void k1_issue_v(const K1Ctx& c) {
  k1_issue_v_piece<T, 0>(c); k1_issue_v_piece<T, 1>(c); k1_issue_v_piece<T, 2>(c); k1_issue_v_piece<T, 3>(c);
}
DI void k1_touch(const K1Ctx& c, __amdgpu_buffer_rsrc_t srd, unsigned soff) {
  asm volatile("buffer_load_dword %0, %1, %2, %3 offen nt" : "+v"(c.warm) : "v"(c.vt), "s"(srd), "s"(soff) : "memory");
}
template <int U> DI void k1_touch_a(const K1Ctx& c) {
  constexpr int ht = U >> 1, t = ht < 4 ? (ht >> 1) : ht - 2;
  constexpr bool isv = ht < 4 && (ht & 1);
  k1_touch(c, isv ? c.sv : c.sk, t * 16384 + (U & 1) * 8192);
}
constexpr int pb_t(int j, int i) { constexpr int t[5][2] = {{2, 4}, {3, 5}, {6, 4}, {7, 5}, {6, 7}}; return t[j][i]; }
constexpr bool pb_v(int j, int i) { constexpr bool v[5][2] = {{true, false}, {true, false}, {false, true}, {false, true}, {true, true}}; return v[j][i]; }
template <int J> DI void k1_touch_b(const K1Ctx& c) {
  if constexpr (J <= 4) {
    const bool lo = c.w4 < 2;
    const __amdgpu_buffer_rsrc_t srd = (lo ? pb_v(J, 0) : pb_v(J, 1)) ? c.sv : c.sk;
    k1_touch(c, srd, (lo ? pb_t(J, 0) : pb_t(J, 1)) * 16384 + (c.w4 & 1) * 8192);
  }
}
template <int J, int I> DI void k1_issue_b1(const K1Ctx& c) {
  if constexpr (J == 0) { if constexpr (I < 4) k1_issue_k_piece<3, I>(c); }
  else if constexpr (J <= 5) {
    if constexpr (pb_v(J - 1, I >> 2)) k1_issue_v_piece<pb_t(J - 1, I >> 2), I & 3>(c);
    else k1_issue_k_piece<pb_t(J - 1, I >> 2), I & 3>(c);
  }
}
template <int J> DI void k1_issue_b(const K1Ctx& c) {
  k1_issue_b1<J, 0>(c); k1_issue_b1<J, 1>(c); k1_issue_b1<J, 2>(c); k1_issue_b1<J, 3>(c);
  k1_issue_b1<J, 4>(c); k1_issue_b1<J, 5>(c); k1_issue_b1<J, 6>(c); k1_issue_b1<J, 7>(c);
  k1_touch_b<J>(c);
}
DI void bar_lgkm() { asm volatile("s_waitcnt lgkmcnt(0)\n\ts_barrier" ::: "memory"); }
#define PIN(x) asm volatile("" : "+v"(x))

struct AFrag { bf16x8 x[2]; s16x4 lo[2][4], hi[2][4]; };
template <int S, int I> DI void k1_read_a1(AFrag& f, lds_cptr (&xb)[2], lds_cptr (&wb)[4]) {
  constexpr int so = A_BASE + (S % 4) * A_SLOT, ks = I / 9, j = I % 9;
  if constexpr (j == 0) f.x[ks] = lds_rd128(xb[ks] + so + A_X);
  else if constexpr ((j - 1) % 2 == 0) f.lo[ks][(j - 1) / 2] = lds_rdtr(wb[(j - 1) / 2] + so + A_W + ks * 8192);
  else f.hi[ks][(j - 1) / 2] = lds_rdtr(wb[(j - 1) / 2] + so + A_W + ks * 8192 + 2048);
}
template <int S, int M>
DI void k1_gap_a(const K1Ctx& c, f32x16 (&qa)[4], const AFrag& fc, AFrag& fn, lds_cptr (&xb)[2], lds_cptr (&wb)[4]) {
  constexpr int ks = M >> 2, dt = M & 3;
  if constexpr (S + 1 < A_STAGES && M < 6) {
    k1_read_a1<S + 1, 3 * M>(fn, xb, wb); k1_read_a1<S + 1, 3 * M + 1>(fn, xb, wb); k1_read_a1<S + 1, 3 * M + 2>(fn, xb, wb);
  }
  qa[dt] = MFMA(cat8(fc.lo[ks][dt], fc.hi[ks][dt]), fc.x[ks], qa[dt]);
  SBAR();
  if constexpr (M == 1) {
    if (c.wid < 4) k1_issue_a<S + 4>(c);
    else {
      if constexpr (S == 7) k1_issue_k<0>(c);
      if constexpr (S == 10) k1_issue_v<0>(c);
      if constexpr (S == 12) k1_issue_k<1>(c);
      if constexpr (S == 14) k1_issue_v<1>(c);
      if constexpr (S == 17) k1_issue_k<2>(c);
    }
    SBAR();
  }
  if constexpr (M == 4 && S < 16) {
    if (c.wid == 4 && c.grp == (unsigned)(S & 3)) { k1_touch_a<3 * (S >> 2)>(c); k1_touch_a<3 * (S >> 2) + 1>(c); k1_touch_a<3 * (S >> 2) + 2>(c); }
    SBAR();
  }
}
template <int S, int... Ms>
DI void k1_stage_a(const K1Ctx& c, f32x16 (&qa)[4], AFrag (&f)[2], lds_cptr (&xb)[2], lds_cptr (&wb)[4], std::integer_sequence<int, Ms...>) {
  if constexpr (S + 1 < A_STAGES) { if (c.wid < 4) wait_vm<a_wait(S + 1)>(); }
  bar_lgkm();
  STAMP(c, 2 + S);
  (k1_gap_a<S, Ms>(c, qa, f[S & 1], f[(S + 1) & 1], xb, wb), ...);
}
template <int... Is> DI void k1_read_a0(AFrag& f, lds_cptr (&xb)[2], lds_cptr (&wb)[4], std::integer_sequence<int, Is...>) {
  (k1_read_a1<0, Is>(f, xb, wb), ...);
}
template <int... Is>
DI void k1_phase_a(const K1Ctx& c, f32x16 (&qa)[4], lds_cptr (&xb)[2], lds_cptr (&wb)[4], std::integer_sequence<int, Is...>) {
  AFrag f[2];
  if (c.wid < 4) wait_vm<a_wait(0)>();
  bar_lgkm();
  STAMP(c, 1);
  k1_read_a0(f[0], xb, wb, std::make_integer_sequence<int, 18>{});
  (k1_stage_a<Is>(c, qa, f, xb, wb, std::make_integer_sequence<int, 8>{}), ...);
}

DI float half_max(float x) {
  auto rr = __builtin_amdgcn_permlane32_swap(__float_as_uint(x), __float_as_uint(x), false, false);
  return fmaxf(__uint_as_float(rr[0]), __uint_as_float(rr[1]));
}
DI float half_sum(float x) {
  auto rr = __builtin_amdgcn_permlane32_swap(__float_as_uint(x), __float_as_uint(x), false, false);
  return __uint_as_float(rr[0]) + __uint_as_float(rr[1]);
}
#define MX3(a, b, c) __builtin_fmaxf(__builtin_fmaxf((a), (b)), (c))

struct AttnState {
  f32x16 o[4];
  f32x16 negm;
  f32x16 s[2][2];
  u32x4 pk[4];
  bf16x8 kf[4];
  s16x4 vlo[4], vhi[4];
  float mref, l;
};

template <int T, int M> DI void k1_kread(AttnState& st, lds_cptr (&kb_)[4]) {
  st.kf[M & 3] = lds_rd128(kb_[M & 3] + k_off(T) + ((M & 7) >> 2) * 8192 + (M >> 3) * 4096);
}
template <int T, int M> DI void k1_vread(AttnState& st, lds_cptr (&vb_)[4]) {
  st.vlo[M & 3] = lds_rdtr(vb_[M & 3] + v_off(T) + (M >> 2) * 4096);
  st.vhi[M & 3] = lds_rdtr(vb_[M & 3] + v_off(T) + (M >> 2) * 4096 + 2048);
}
template <int J> DI void k1_bar_b(const K1Ctx& c) {
  if (c.wid >= 4) wait_vm<b_wait(J)>();
  if constexpr (J == KV_TILES) asm volatile("" :: "v"(c.warm));
  if constexpr (J == 0) bar_lgkm();
  else asm volatile("s_barrier" ::: "memory");
  STAMP(c, 20 + J);
}

template <int T, int M>
DI void k1_gap_qk(AttnState& st, const bf16x8 (&q)[8], lds_cptr (&kb_)[4], lds_cptr (&vb_)[4], float& sacc, float& rm) {
  constexpr int X = T & 1, Y = X ^ 1, kb = M >> 3, ds = M & 7;
  const bf16x8 kf = st.kf[M & 3];
  if constexpr (ds == 0) {
    if constexpr (T == 0) { f32x16 z = {}; st.s[X][kb] = MFMA(kf, q[ds], z); }
    else st.s[X][kb] = MFMA(kf, q[ds], st.negm);
  } else st.s[X][kb] = MFMA(kf, q[ds], st.s[X][kb]);
  if constexpr (M + 4 < 16) k1_kread<T, M + 4>(st, kb_);
  if constexpr (T > 0) {
    if constexpr (M >= 12) k1_vread<T - 1, M - 12>(st, vb_);
    const f32x16& P = st.s[Y][M >> 3];
    constexpr int e = (M & 7) * 2;
    sacc += P[e]; sacc += P[e + 1];
    st.pk[M >> 2][M & 3] = cvtpk(P[e], P[e + 1]);
    PIN(sacc); PIN(st.pk[M >> 2]);
  }
  if constexpr (M >= 10 && M < 14) {
    constexpr int e = (M - 10) * 4;
    const f32x16& S0 = st.s[X][0];
    if constexpr (M == 10) rm = MX3(S0[0], S0[1], S0[2]); else rm = MX3(rm, S0[e - 1], S0[e]);
    if constexpr (M == 10) rm = fmaxf(rm, S0[3]);
    else rm = MX3(rm, S0[e + 1], S0[e + 2]);
    if constexpr (M == 13) rm = fmaxf(rm, S0[15]);
    PIN(rm);
  }
  SBAR();
}
template <int T, int... Ms>
DI void k1_qk(AttnState& st, const bf16x8 (&q)[8], lds_cptr (&kb_)[4], lds_cptr (&vb_)[4], float& sacc, float& rm,
              std::integer_sequence<int, Ms...>) {
  (k1_gap_qk<T, Ms>(st, q, kb_, vb_, sacc, rm), ...);
}
template <int T, int M, int E0, int E1>
DI void k1_gap_pv(const K1Ctx& c, AttnState& st, lds_cptr (&kb_)[4], lds_cptr (&vb_)[4]) {
  constexpr int X = T & 1;
  st.o[M & 3] = MFMA(cat8(st.vlo[M & 3], st.vhi[M & 3]), __builtin_bit_cast(bf16x8, st.pk[M >> 2]), st.o[M & 3]);
  if constexpr (M + 4 < 16) k1_vread<T - 1, M + 4>(st, vb_);
  if constexpr (M >= 12) {
    if constexpr (T + 1 < KV_TILES) k1_kread<T + 1, M - 12>(st, kb_);
    else k1_vread<T, M - 12>(st, vb_);
  }
#pragma unroll
  for (int e = E0; e < E1; ++e) st.s[X][e >> 4][e & 15] = __builtin_amdgcn_exp2f(st.s[X][e >> 4][e & 15]);
  if constexpr (E0 < E1) { PIN(st.s[X][E0 >> 4]); if constexpr (((E1 - 1) >> 4) != (E0 >> 4)) PIN(st.s[X][(E1 - 1) >> 4]); }
  SBAR();
  if constexpr (T + 1 <= 5 && M >= 2 && M < 10) { if (c.wid >= 4) k1_issue_b1<T + 1, M - 2>(c); SBAR(); }
  if constexpr (T + 1 <= 4 && M == 10) { if (c.wid >= 4) k1_touch_b<T + 1>(c); SBAR(); }
}
constexpr int pv_e(int m) { return m < 2 ? 0 : m >= 16 ? 32 : ((m - 2) * 32 + 6) / 14; }
template <int T, int... Ms>
DI void k1_pv_tail(const K1Ctx& c, AttnState& st, lds_cptr (&kb_)[4], lds_cptr (&vb_)[4], std::integer_sequence<int, Ms...>) {
  (k1_gap_pv<T, Ms + 2, pv_e(Ms + 2), pv_e(Ms + 3)>(c, st, kb_, vb_), ...);
}

template <int T>
DI void k1_step_b(const K1Ctx& c, AttnState& st, const bf16x8 (&q)[8], lds_cptr (&kb_)[4], lds_cptr (&vb_)[4]) {
  constexpr int X = T & 1;
  float sacc = 0.f, rm = 0.f;
  k1_qk<T>(st, q, kb_, vb_, sacc, rm, std::make_integer_sequence<int, 16>{});
  st.l += sacc;
  k1_bar_b<T + 1>(c);
  if constexpr (T == 0) {
    if (c.wid >= 4) k1_issue_b<T + 1>(c);
    SBAR();
    k1_kread<1, 0>(st, kb_); k1_kread<1, 1>(st, kb_); k1_kread<1, 2>(st, kb_); k1_kread<1, 3>(st, kb_);
#pragma unroll
    for (int r = 0; r < 16; r += 2) rm = MX3(rm, st.s[X][1][r], st.s[X][1][r + 1]);
    rm = half_max(rm);
    st.mref = rm;
#pragma unroll
    for (int r = 0; r < 16; ++r) {
      st.s[X][0][r] = __builtin_amdgcn_exp2f(st.s[X][0][r] - rm);
      st.s[X][1][r] = __builtin_amdgcn_exp2f(st.s[X][1][r] - rm);
      st.negm[r] = -rm;
    }
    SBAR();
  } else {
    k1_gap_pv<T, 0, 0, 0>(c, st, kb_, vb_);
#pragma unroll
    for (int r = 0; r < 16; r += 2) rm = MX3(rm, st.s[X][1][r], st.s[X][1][r + 1]);
    PIN(rm);
    k1_gap_pv<T, 1, 0, 0>(c, st, kb_, vb_);
    rm = half_max(rm);
    bool resc = false;
    float f = 1.f;
    if (__builtin_expect(__any(rm > THR), 0)) {
      const float dl = fmaxf(rm, 0.f);
      st.mref += dl;
      f = __builtin_amdgcn_exp2f(-dl);
      st.l *= f;
#pragma unroll
      for (int r = 0; r < 16; ++r) { st.s[X][0][r] -= dl; st.s[X][1][r] -= dl; st.negm[r] = -st.mref; }
      resc = true;
    }
    SBAR();
    k1_pv_tail<T>(c, st, kb_, vb_, std::make_integer_sequence<int, 14>{});
    if (__builtin_expect(resc, 0)) {
#pragma unroll
      for (int dt = 0; dt < 4; ++dt)
#pragma unroll
        for (int r = 0; r < 16; ++r) st.o[dt][r] *= f;
    }
    SBAR();
  }
}
template <int M> DI void k1_gap_last(AttnState& st, lds_cptr (&vb_)[4]) {
  constexpr int T = KV_TILES - 1;
  st.o[M & 3] = MFMA(cat8(st.vlo[M & 3], st.vhi[M & 3]), __builtin_bit_cast(bf16x8, st.pk[M >> 2]), st.o[M & 3]);
  if constexpr (M + 4 < 16) k1_vread<T, M + 4>(st, vb_);
  SBAR();
}
template <int... Ms> DI void k1_last(AttnState& st, lds_cptr (&vb_)[4], std::integer_sequence<int, Ms...>) {
  constexpr int X = (KV_TILES - 1) & 1;
  float sacc = 0.f;
#pragma unroll
  for (int kb = 0; kb < 2; ++kb)
#pragma unroll
    for (int e = 0; e < 16; e += 2) {
      sacc += st.s[X][kb][e]; sacc += st.s[X][kb][e + 1];
      st.pk[kb * 2 + (e >> 3)][(e >> 1) & 3] = cvtpk(st.s[X][kb][e], st.s[X][kb][e + 1]);
    }
  st.l += sacc;
  SBAR();
  (k1_gap_last<Ms>(st, vb_), ...);
}
template <int... Is>
DI void k1_phase_b(const K1Ctx& c, AttnState& st, const bf16x8 (&q)[8], lds_cptr (&kb_)[4], lds_cptr (&vb_)[4],
                   std::integer_sequence<int, Is...>) {
  k1_bar_b<0>(c);
  k1_kread<0, 0>(st, kb_); k1_kread<0, 1>(st, kb_); k1_kread<0, 2>(st, kb_); k1_kread<0, 3>(st, kb_);
  SBAR();
  if (c.wid >= 4) k1_issue_b<0>(c);
  SBAR();
  (k1_step_b<Is>(c, st, q, kb_, vb_), ...);
  k1_last(st, vb_, std::make_integer_sequence<int, 16>{});
}

__global__ __launch_bounds__(512, 2) void k1_attn(const __hip_bfloat16* __restrict__ xA, const __hip_bfloat16* __restrict__ kA,
                                                  const __hip_bfloat16* __restrict__ vA, const __hip_bfloat16* __restrict__ wqA,
                                                  const __hip_bfloat16* __restrict__ xB, const __hip_bfloat16* __restrict__ kB,
                                                  const __hip_bfloat16* __restrict__ vB, const __hip_bfloat16* __restrict__ wqB,
                                                  u32x4* __restrict__ ofrag) {
  extern __shared__ __attribute__((aligned(16))) char lds[];
  const int tid = threadIdx.x, lane = tid & 63;
  const unsigned wid = __builtin_amdgcn_readfirstlane(tid >> 6);
  const int bid = blockIdx.x, xcd = bid & 7, idx = bid >> 3;
  const int p = xcd >> 2, kvh = (xcd & 3) * 4 + (idx & 3), b = idx >> 2;
  const int hh = wid >> 2, rb = wid & 3;
  const int r = lane & 31, h = lane >> 5, blk = (lane >> 4) & 1, qq = (lane & 15) >> 2, pp = lane & 3;

  K1Ctx c;
  STAMP_INIT(c); STAMP(c, 0);
  c.ldsb = (unsigned)(uintptr_t)lds;
  c.wid = wid;
  {
    const __hip_bfloat16* x = p ? xB : xA; const __hip_bfloat16* wq = p ? wqB : wqA;
    const __hip_bfloat16* kc = p ? kB : kA; const __hip_bfloat16* vc = p ? vB : vA;
    c.sx = make_srd(x + (size_t)b * SQ * DM, SQ * DM * 2);
    c.sw = make_srd(wq + kvh * 256, (DM * NQ - kvh * 256) * 2);
    c.sk = make_srd(kc + (size_t)(b * HKV + kvh) * SK * DH, SK * DH * 2);
    c.sv = make_srd(vc + (size_t)(b * HKV + kvh) * SK * DH, SK * DH * 2);
  }
  c.w4 = wid & 3;
  c.warm = 0;
  c.grp = idx & 3;
  c.vt = lane * 128;
  if (wid < 4) {
    const unsigned xr = 32 * c.w4 + (lane >> 2);
    c.v0 = xr * (DM * 2) + (((lane & 3) ^ ((xr >> 2) & 3)) << 4);
    const unsigned k0 = 8 * c.w4 + (lane >> 5), k1 = k0 + 2;
    c.v1 = k0 * 8192 + (((lane & 31) ^ ((k0 & 3) << 2)) << 4);
    c.v2 = k1 * 8192 + (((lane & 31) ^ ((k1 & 3) << 2)) << 4);
    k1_issue_a<0>(c); k1_issue_a<1>(c); k1_issue_a<2>(c); k1_issue_a<3>(c);
  } else {
    const unsigned kr0 = 16 * c.w4 + (lane >> 3), kr1 = kr0 + 8;
    c.v0 = kr0 * 256 + (((lane & 7) ^ ((kr0 >> 1) & 7)) << 4);
    c.v1 = kr1 * 256 + (((lane & 7) ^ ((kr1 >> 1) & 7)) << 4);
    const unsigned vr = 16 * c.w4 + (lane >> 4);
    c.v2 = vr * 256 + (((lane & 15) ^ ((vr & 3) << 2)) << 4);
    {
      const char* wqg = (const char*)(p ? wqB : wqA) + (xcd & 3) * 2048;
      const char* xg = (const char*)(p ? xB : xA);
      {
        const int krow = c.w4 * 128 + 4 * idx + (lane >> 4);
        const char* a = wqg + (size_t)krow * 8192 + (lane & 15) * 128;
        WARM_LOAD(c.warm, a);
      }
      {
        const int f = c.w4 * 64 + lane;
        const int ff = f < 252 ? f : 251;
        const char* a = xg + (size_t)(idx * 28 + ff % 28) * (DM * 2) + (ff / 28) * 128;
        WARM_LOAD(c.warm, a);
      }
      if (c.w4 == 0 && idx < 16) {
        const int krow = 512 + 4 * idx + (lane >> 4);
        const char* a = wqg + (size_t)krow * 8192 + (lane & 15) * 128;
        WARM_LOAD(c.warm, a);
      }
    }
  }

  const lds_cptr L = (lds_cptr)lds;
  const int xrow = rb * 32 + r;
  lds_cptr xb[2];
  xb[0] = L + xrow * 64 + ((h ^ ((xrow >> 2) & 3)) << 4);
  xb[1] = L + xrow * 64 + (((2 + h) ^ ((xrow >> 2) & 3)) << 4);
  lds_cptr wb[4];
  {
    const int psw = ((pp & 1) << 1) | (pp >> 1);
#pragma unroll
    for (int dt = 0; dt < 4; ++dt) wb[dt] = L + (8 * h + qq) * 512 + hh * 256 + ((dt ^ qq) << 6) + blk * 32 + psw * 8;
  }
  f32x16 qa[4] = {};
  k1_phase_a(c, qa, xb, wb, std::make_integer_sequence<int, A_STAGES>{});

  bf16x8 q[8];
#pragma unroll
  for (int dt = 0; dt < 4; ++dt) {
#pragma unroll
    for (int i = 0; i < 16; ++i) qa[dt][i] *= QSCALE;
    q[2 * dt] = pack8(qa[dt], 0);
    q[2 * dt + 1] = pack8(qa[dt], 1);
  }

  lds_cptr kb_[4], vb_[4];
#pragma unroll
  for (int j = 0; j < 4; ++j) {
    kb_[j] = L + r * 128 + (((2 * j + h) ^ ((r >> 1) & 7)) << 4);
    vb_[j] = L + (4 * h + qq) * 256 + ((j ^ qq) << 6) + blk * 32 + pp * 8;
  }
  AttnState st;
#pragma unroll
  for (int dt = 0; dt < 4; ++dt) st.o[dt] = f32x16{};
  st.negm = f32x16{}; st.mref = 0.f; st.l = 0.f;
  k1_phase_b(c, st, q, kb_, vb_, std::make_integer_sequence<int, KV_TILES>{});

  STAMP(c, 30);
  const float lt = half_sum(st.l);
  const float inv = 1.0f / lt;
  if (xrow < SQ) {
    const int g = b * SQ + xrow;
    const int head = kvh * 2 + hh;
    u32x4* dst = ofrag + ((size_t)((p * 28 + (g >> 5)) * 256 + head * 8)) * 64 + h * 32 + (g & 31);
#pragma unroll
    for (int dt = 0; dt < 4; ++dt) {
#pragma unroll
      for (int i = 0; i < 16; ++i) st.o[dt][i] *= inv;
      dst[(2 * dt) * 64] = __builtin_bit_cast(u32x4, pack8(st.o[dt], 0));
      dst[(2 * dt + 1) * 64] = __builtin_bit_cast(u32x4, pack8(st.o[dt], 1));
    }
  }
  STAMP(c, 31);
  STAMP_OUT(c, ofrag + (32u << 20) / 16);
}

#define K2_O_NT 0
#define K2_W_NT 0
#define K2_DEPTH 5
#define K2_IL 0
constexpr int K2_D = K2_DEPTH, K2_STEPS = 32, K2_LDS = 163840;
constexpr int K2_KS = K2_IL ? 8 : 1;

struct K2Ctx {
  __amdgpu_buffer_rsrc_t so, sw;
  unsigned ring, va, vb, soa, sob;
  STAMP_MEMBER
};
template <int I> DI void k2_issue(const K2Ctx& c) {
  if constexpr (I < K2_STEPS) {
    const unsigned sb = c.ring + (I % K2_D) * 4096;
    dma16<0, K2_O_NT>(sb, c.va, c.so, c.soa + I * K2_KS * 1024);
    dma16<0, K2_O_NT>(sb + 1024, c.va, c.so, c.soa + I * K2_KS * 1024 + 256 * 1024);
    dma16<0, K2_W_NT>(sb + 2048, c.vb, c.sw, c.sob + I * K2_KS * 16 * DM * 2);
    dma16<0, K2_W_NT>(sb + 3072, c.vb, c.sw, c.sob + (I * K2_KS * 16 + 8) * DM * 2);
  }
}
template <int... Is> DI void k2_prologue(const K2Ctx& c, std::integer_sequence<int, Is...>) { (k2_issue<Is>(c), ...); }
constexpr int k2_wait(int i) { return 4 * ((K2_STEPS - 1 - i) < (K2_D - 1) ? (K2_STEPS - 1 - i) : (K2_D - 1)); }

template <int I>
DI void k2_step(const K2Ctx& c, f32x16 (&acc)[2][2], lds_cptr ab, lds_cptr (&bb)[2]) {
  wait_vm<k2_wait(I)>();
  STAMP(c, 1 + I);
  constexpr int so = (I % K2_D) * 4096;
  bf16x8 of[2], wf[2];
  of[0] = lds_rd128(ab + so);
  of[1] = lds_rd128(ab + so + 1024);
#pragma unroll
  for (int nb = 0; nb < 2; ++nb) wf[nb] = cat8(lds_rdtr(bb[nb] + so + 2048), lds_rdtr(bb[nb] + so + 3072));
  wait_lgkm0();
  SBAR();
  k2_issue<I + K2_D>(c);
#pragma unroll
  for (int nb = 0; nb < 2; ++nb)
#pragma unroll
    for (int mb = 0; mb < 2; ++mb) acc[nb][mb] = MFMA(wf[nb], of[mb], acc[nb][mb]);
}
template <int... Is>
DI void k2_loop(const K2Ctx& c, f32x16 (&acc)[2][2], lds_cptr ab, lds_cptr (&bb)[2], std::integer_sequence<int, Is...>) {
  (k2_step<Is>(c, acc, ab, bb), ...);
}

__global__ __launch_bounds__(512, 2) void k2_oproj(const u32x4* __restrict__ ofrag, const __hip_bfloat16* __restrict__ woA,
                                                   const __hip_bfloat16* __restrict__ woB, __hip_bfloat16* __restrict__ out) {
  extern __shared__ __attribute__((aligned(16))) char lds[];
  const int tid = threadIdx.x, lane = tid & 63;
  const unsigned wid = __builtin_amdgcn_readfirstlane(tid >> 6);
  const int bid = blockIdx.x, xcd = bid & 7, idx = bid >> 3;
  const int p = xcd >> 2, q4 = xcd & 3;
  const int t0 = (126 * q4) >> 2, t1 = (126 * (q4 + 1)) >> 2;
  const int tile = t0 + idx;
  if (tile >= t1) return;
  const int mt = tile / 9, nt = tile - mt * 9;
  const int r = lane & 31, h = lane >> 5, blk = (lane >> 4) & 1, qq = (lane & 15) >> 2, pp = lane & 3;

  K2Ctx c;
  STAMP_INIT(c); STAMP(c, 0);
  c.ring = (unsigned)(uintptr_t)lds + wid * (K2_D * 4096);
  c.so = make_srd(ofrag + (size_t)((p * 28 + 2 * mt) * 256) * 64, 2 * 256 * 1024);
  c.sw = make_srd((p ? woB : woA) + nt * 64, (NQ * DM - nt * 64) * 2);
  c.va = lane * 16;
  {
    const unsigned kr = lane >> 3;
    c.vb = kr * (DM * 2) + (((lane & 7) ^ (((kr >> 1) & 1) << 2)) << 4);
  }
  c.soa = wid * (K2_IL ? 1 : K2_STEPS) * 1024;
  c.sob = wid * (K2_IL ? 1 : K2_STEPS) * 16 * DM * 2;
  k2_prologue(c, std::make_integer_sequence<int, K2_D>{});

  const lds_cptr R = (lds_cptr)lds + wid * (K2_D * 4096);
  lds_cptr ab = R + lane * 16;
  lds_cptr bb[2];
#pragma unroll
  for (int nb = 0; nb < 2; ++nb) bb[nb] = R + (4 * h + qq) * 128 + ((nb ^ ((qq >> 1) & 1)) << 6) + blk * 32 + pp * 8;
  f32x16 acc[2][2] = {};
  k2_loop(c, acc, ab, bb, std::make_integer_sequence<int, K2_STEPS>{});

  STAMP(c, 33);
  __syncthreads();
  f32x4* red = (f32x4*)lds;
#pragma unroll
  for (int nb = 0; nb < 2; ++nb)
#pragma unroll
    for (int mb = 0; mb < 2; ++mb)
#pragma unroll
      for (int g = 0; g < 4; ++g) {
        const f32x4 v = {acc[nb][mb][4 * g], acc[nb][mb][4 * g + 1], acc[nb][mb][4 * g + 2], acc[nb][mb][4 * g + 3]};
        red[(wid * 16 + (nb * 2 + mb) * 4 + g) * 64 + lane] = v;
      }
  __syncthreads();
#pragma unroll
  for (int j = 0; j < 2; ++j) {
    const int Q = wid * 2 + j;
    f32x4 s = red[Q * 64 + lane];
#pragma unroll
    for (int w = 1; w < 8; ++w) s += red[(w * 16 + Q) * 64 + lane];
    const int nb = Q >> 3, mb = (Q >> 2) & 1, g = Q & 3;
    const int row = mt * 64 + mb * 32 + r;
    const int n = nt * 64 + nb * 32 + 8 * g + 4 * h;
    u32x2 o = {cvtpk(s[0], s[1]), cvtpk(s[2], s[3])};
    *(u32x2*)(out + (size_t)row * (2 * DM) + p * DM + n) = o;
  }
  STAMP(c, 34);
  STAMP_OUT(c, (u32x4*)ofrag + (48u << 20) / 16);
}

extern "C" void kernel_launch(void* const* d_in, const int* in_sizes, int n_in,
                              void* d_out, int out_size, void* d_ws, size_t ws_size,
                              hipStream_t stream) {
  const __hip_bfloat16* xA = (const __hip_bfloat16*)d_in[0];
  const __hip_bfloat16* kA = (const __hip_bfloat16*)d_in[1];
  const __hip_bfloat16* vA = (const __hip_bfloat16*)d_in[2];
  const __hip_bfloat16* wqA = (const __hip_bfloat16*)d_in[3];
  const __hip_bfloat16* woA = (const __hip_bfloat16*)d_in[4];
  const __hip_bfloat16* xB = (const __hip_bfloat16*)d_in[5];
  const __hip_bfloat16* kB = (const __hip_bfloat16*)d_in[6];
  const __hip_bfloat16* vB = (const __hip_bfloat16*)d_in[7];
  const __hip_bfloat16* wqB = (const __hip_bfloat16*)d_in[8];
  const __hip_bfloat16* woB = (const __hip_bfloat16*)d_in[9];
  u32x4* ofrag = (u32x4*)d_ws;
  hipFuncSetAttribute((const void*)k1_attn, hipFuncAttributeMaxDynamicSharedMemorySize, K1_LDS);
  hipFuncSetAttribute((const void*)k2_oproj, hipFuncAttributeMaxDynamicSharedMemorySize, K2_LDS);
  hipLaunchKernelGGL(k1_attn, dim3(256), dim3(512), K1_LDS, stream, xA, kA, vA, wqA, xB, kB, vB, wqB, ofrag);
  hipLaunchKernelGGL(k2_oproj, dim3(256), dim3(512), K2_LDS, stream, (const u32x4*)ofrag, woA, woB, (__hip_bfloat16*)d_out);
}
```

```cpp
#include <hip/hip_runtime.h>
#include <hip/hip_bf16.h>
#include <stdint.h>
#include <utility>

typedef short bf16x8 __attribute__((ext_vector_type(8)));
typedef short s16x4 __attribute__((ext_vector_type(4)));
typedef float f32x16 __attribute__((ext_vector_type(16)));
typedef float f32x4 __attribute__((ext_vector_type(4)));
typedef float f32x2 __attribute__((ext_vector_type(2)));
typedef __bf16 bf16x2_t __attribute__((ext_vector_type(2)));
typedef unsigned u32x4 __attribute__((ext_vector_type(4)));
typedef unsigned u32x2 __attribute__((ext_vector_type(2)));
typedef __attribute__((address_space(3))) const char* lds_cptr;
typedef short v4i16_t __attribute__((ext_vector_type(4)));

#define DI __device__ __forceinline__
#define SBAR() __builtin_amdgcn_sched_barrier(0)
#define MFMA(a, b, c) __builtin_amdgcn_mfma_f32_32x32x16_bf16((a), (b), (c), 0, 0, 0)

constexpr int NB = 8, SQ = 112, DM = 576, HQ = 32, HKV = 16, SK = 512, DH = 128;
constexpr int NQ = HQ * DH;
constexpr float QSCALE = 0.088388347648318447f * 1.4426950408889634f;
constexpr float THR = 8.0f;

DI unsigned cvtpk(float lo, float hi) {
  f32x2 v = {lo, hi};
  bf16x2_t b = __builtin_convertvector(v, bf16x2_t);
  return __builtin_bit_cast(unsigned, b);
}
DI bf16x8 pack8(const f32x16& x, int s) {
  u32x4 w = {cvtpk(x[8 * s + 0], x[8 * s + 1]), cvtpk(x[8 * s + 2], x[8 * s + 3]),
             cvtpk(x[8 * s + 4], x[8 * s + 5]), cvtpk(x[8 * s + 6], x[8 * s + 7])};
  return __builtin_bit_cast(bf16x8, w);
}
DI bf16x8 lds_rd128(lds_cptr p) { return *(const __attribute__((address_space(3))) bf16x8*)p; }
DI s16x4 lds_rdtr(lds_cptr p) {
  return __builtin_bit_cast(s16x4, __builtin_amdgcn_ds_read_tr16_b64_v4i16((__attribute__((address_space(3))) v4i16_t*)p));
}
DI bf16x8 cat8(s16x4 lo, s16x4 hi) { return __builtin_shufflevector(lo, hi, 0, 1, 2, 3, 4, 5, 6, 7); }

template <int OFF, int NT = 1>
DI void dma16(unsigned m0v, unsigned voff, __amdgpu_buffer_rsrc_t srd, unsigned soff) {
  unsigned keep;
  if constexpr (NT == 1)
    asm volatile("s_mov_b32 %0, m0\n\ts_mov_b32 m0, %1\n\ts_nop 0\n\t"
                 "buffer_load_dwordx4 %2, %3, %4 offen offset:%5 nt lds\n\ts_mov_b32 m0, %0"
                 : "=&s"(keep) : "s"(m0v), "v"(voff), "s"(srd), "s"(soff), "i"(OFF) : "memory");
  else
    asm volatile("s_mov_b32 %0, m0\n\ts_mov_b32 m0, %1\n\ts_nop 0\n\t"
                 "buffer_load_dwordx4 %2, %3, %4 offen offset:%5 lds\n\ts_mov_b32 m0, %0"
                 : "=&s"(keep) : "s"(m0v), "v"(voff), "s"(srd), "s"(soff), "i"(OFF) : "memory");
}
template <int N> DI void wait_vm() { asm volatile("s_waitcnt vmcnt(%0)" :: "i"(N) : "memory"); }
DI void wait_lgkm0() { asm volatile("s_waitcnt lgkmcnt(0)" ::: "memory"); }

DI unsigned fresh_lane() {
  unsigned l;
  asm volatile("v_mbcnt_lo_u32_b32 %0, -1, 0\n\tv_mbcnt_hi_u32_b32 %0, -1, %0" : "=v"(l));
  return l;
}
DI __amdgpu_buffer_rsrc_t make_srd(const void* base, unsigned bytes) {
  return __builtin_amdgcn_make_buffer_rsrc((void*)base, (short)0, (int)bytes, 0x00020000);
}

constexpr int KV_NT = 1, A_NT = 0;
constexpr int A_STAGES = 18;
constexpr int A_SLOT = 24576, A_BASE = 65536, A_X = 0, A_W = 8192;
constexpr int KV_TILES = 8;
constexpr int WO_STEP = 3;
constexpr int K1_LDS = 163840;
constexpr int k_off(int t) { constexpr int h[8] = {0, 2, 7, 8, 4, 6, 0, 7}; return h[t] * 16384; }
constexpr int v_off(int t) { constexpr int h[8] = {1, 3, 9, 5, 2, 1, 8, 3}; return h[t] * 16384; }
constexpr int a_wait(int s) { return s == 0 ? 9 : s == A_STAGES - 1 ? 4 : s == A_STAGES - 2 ? 5 : 6; }
constexpr int b_wait(int j) { constexpr int n[9] = {8, 12, 12, 10, 10, 8, 6, 10, 12}; return n[j]; }
constexpr int w_off(int j) { constexpr int h[8] = {9, 4, 5, 6, 2, 0, 1, 7}; return h[j] * 16384; }

struct K1Ctx {
  __amdgpu_buffer_rsrc_t sx, sw, sk, sv, swo;
  __amdgpu_buffer_rsrc_t swt;
  unsigned wbuf; mutable unsigned vwo;
  unsigned wo_q, wo_off;
  mutable unsigned warm2;
  unsigned ldsb, wid, grp;
  unsigned vx, vw0, vw1, vk, vv;
};

template <int S> DI void k1_issue_a(const K1Ctx& c) {
  if constexpr (S < A_STAGES) {
    const unsigned sb = c.ldsb + A_BASE + (S % 4) * A_SLOT;
    dma16<0, A_NT>(sb + A_X + c.wid * 1024, c.vx, c.sx, S * 64);
    dma16<0, A_NT>(sb + A_W + c.wid * 2048, c.vw0, c.sw, S * 32 * 8192);
    dma16<0, A_NT>(sb + A_W + c.wid * 2048 + 1024, c.vw1, c.sw, S * 32 * 8192);
  }
}
template <int T, int P> DI void k1_issue_k_piece(const K1Ctx& c) {
  dma16<0, KV_NT>(c.ldsb + k_off(T) + P * 8192 + c.wid * 1024, c.vk, c.sk, T * 16384 + P * 128);
}
template <int T, int P> DI void k1_issue_v_piece(const K1Ctx& c) {
  dma16<P * 1024, KV_NT>(c.ldsb + v_off(T) + c.wid * 2048, c.vv, c.sv, T * 16384);
}
template <int T> DI void k1_issue_k(const K1Ctx& c) { k1_issue_k_piece<T, 0>(c); k1_issue_k_piece<T, 1>(c); }
template <int T> DI void k1_issue_v(const K1Ctx& c) { k1_issue_v_piece<T, 0>(c); k1_issue_v_piece<T, 1>(c); }
template <int J, int I> DI void k1_issue_b1(const K1Ctx& c) {
  if constexpr (J <= 4 && (I & 1) == 0 && ((J != 0 && J != 2) || (I & 3) == 0)) {
    if constexpr (J == 0) k1_issue_v_piece<3, (I >> 2)>(c);
    if constexpr (J == 1) { if constexpr (I < 4) k1_issue_k_piece<5, (I >> 1)>(c); else k1_issue_k_piece<6, ((I >> 1) & 1)>(c); }
    if constexpr (J == 2) k1_issue_v_piece<4, (I >> 2)>(c);
    if constexpr (J == 3) { if constexpr (I < 4) k1_issue_v_piece<5, (I >> 1)>(c); else k1_issue_k_piece<7, ((I >> 1) & 1)>(c); }
    if constexpr (J == 4) { if constexpr (I < 4) k1_issue_v_piece<6, (I >> 1)>(c); else k1_issue_v_piece<7, ((I >> 1) & 1)>(c); }
    SBAR();
  }
}
template <int I> DI void k1_touch_wo(const K1Ctx& c) {
  const unsigned ln = ((2 * I + (c.wid & 1)) * 32 + c.wo_q) * 64 + fresh_lane();
  const unsigned row = ln / 5, vo = row * (DM * 2) + (ln - row * 5) * 128;
  if (c.wid < 2)
    asm volatile("buffer_load_dword %0, %1, %2, %3 offen" : "+v"(c.warm2) : "v"(vo), "s"(c.swo), "s"(c.wo_off) : "memory");
  SBAR();
}
template <int I, int HALF> DI void k2_issue_w(const K1Ctx& c) {
  dma16<0, 0>(c.wbuf + w_off(I & 7) + HALF * 1024, c.vwo, c.swt, ((32 * c.wid + I) * 16 + 8 * HALF) * (DM * 2));
}
template <int G> DI void k2_issue_wgroup(const K1Ctx& c) {
  k2_issue_w<8 * G + 0, 0>(c); k2_issue_w<8 * G + 0, 1>(c); k2_issue_w<8 * G + 1, 0>(c); k2_issue_w<8 * G + 1, 1>(c);
  k2_issue_w<8 * G + 2, 0>(c); k2_issue_w<8 * G + 2, 1>(c); k2_issue_w<8 * G + 3, 0>(c); k2_issue_w<8 * G + 3, 1>(c);
  k2_issue_w<8 * G + 4, 0>(c); k2_issue_w<8 * G + 4, 1>(c); k2_issue_w<8 * G + 5, 0>(c); k2_issue_w<8 * G + 5, 1>(c);
  k2_issue_w<8 * G + 6, 0>(c); k2_issue_w<8 * G + 6, 1>(c); k2_issue_w<8 * G + 7, 0>(c); k2_issue_w<8 * G + 7, 1>(c);
}
DI void bar_lgkm() { asm volatile("s_waitcnt lgkmcnt(0)\n\ts_barrier" ::: "memory"); }
#define PIN(x) asm volatile("" : "+v"(x))

struct AFrag { bf16x8 x[2]; s16x4 lo[2][4], hi[2][4]; };
template <int S, int I> DI void k1_read_a1(AFrag& f, lds_cptr (&xb)[2], lds_cptr (&wb)[4]) {
  constexpr int so = A_BASE + (S % 4) * A_SLOT, ks = I / 9, j = I % 9;
  if constexpr (j == 0) f.x[ks] = lds_rd128(xb[ks] + so + A_X);
  else if constexpr ((j - 1) % 2 == 0) f.lo[ks][(j - 1) / 2] = lds_rdtr(wb[(j - 1) / 2] + so + A_W + ks * 8192);
  else f.hi[ks][(j - 1) / 2] = lds_rdtr(wb[(j - 1) / 2] + so + A_W + ks * 8192 + 2048);
}
template <int S, int M>
DI void k1_gap_a(const K1Ctx& c, f32x16 (&qa)[4], const AFrag& fc, AFrag& fn, lds_cptr (&xb)[2], lds_cptr (&wb)[4]) {
  constexpr int ks = M >> 2, dt = M & 3;
  if constexpr (S + 1 < A_STAGES && M < 6) {
    k1_read_a1<S + 1, 3 * M>(fn, xb, wb); k1_read_a1<S + 1, 3 * M + 1>(fn, xb, wb); k1_read_a1<S + 1, 3 * M + 2>(fn, xb, wb);
  }
  qa[dt] = MFMA(cat8(fc.lo[ks][dt], fc.hi[ks][dt]), fc.x[ks], qa[dt]);
  SBAR();
  if constexpr (M == 1) { k1_issue_a<S + 4>(c); SBAR(); }
  if constexpr (M == 4) {
    if constexpr (S >= 1 && S < 5) { if (c.grp == S - 1) k1_issue_k<0>(c); }
    if constexpr (S >= 5 && S < 9) { if (c.grp == S - 5) k1_issue_v<0>(c); }
    if constexpr (S >= 8 && S < 12) { if (c.grp == S - 8) k1_issue_k<1>(c); }
    if constexpr (S >= 11 && S < 15) { if (c.grp == S - 11) k1_issue_v<1>(c); }
    if constexpr (S == 14) k1_issue_k<2>(c);
    if constexpr (S == 15) k1_issue_k<3>(c);
    if constexpr (S == 16) k1_issue_v<2>(c);
    if constexpr (S == 17) k1_issue_k<4>(c);
    SBAR();
  }
}
template <int S, int... Ms>
DI void k1_stage_a(const K1Ctx& c, f32x16 (&qa)[4], AFrag (&f)[2], lds_cptr (&xb)[2], lds_cptr (&wb)[4], std::integer_sequence<int, Ms...>) {
  if constexpr (S + 1 < A_STAGES) wait_vm<a_wait(S + 1)>();
  bar_lgkm();
  (k1_gap_a<S, Ms>(c, qa, f[S & 1], f[(S + 1) & 1], xb, wb), ...);
}
template <int... Is> DI void k1_read_a0(AFrag& f, lds_cptr (&xb)[2], lds_cptr (&wb)[4], std::integer_sequence<int, Is...>) {
  (k1_read_a1<0, Is>(f, xb, wb), ...);
}
template <int... Is>
DI void k1_phase_a(const K1Ctx& c, f32x16 (&qa)[4], lds_cptr (&xb)[2], lds_cptr (&wb)[4], std::integer_sequence<int, Is...>) {
  AFrag f[2];
  wait_vm<a_wait(0)>();
  bar_lgkm();
  k1_read_a0(f[0], xb, wb, std::make_integer_sequence<int, 18>{});
  (k1_stage_a<Is>(c, qa, f, xb, wb, std::make_integer_sequence<int, 8>{}), ...);
}

DI float half_max(float x) {
  auto rr = __builtin_amdgcn_permlane32_swap(__float_as_uint(x), __float_as_uint(x), false, false);
  return fmaxf(__uint_as_float(rr[0]), __uint_as_float(rr[1]));
}
DI float half_sum(float x) {
  auto rr = __builtin_amdgcn_permlane32_swap(__float_as_uint(x), __float_as_uint(x), false, false);
  return __uint_as_float(rr[0]) + __uint_as_float(rr[1]);
}
#define MX3(a, b, c) __builtin_fmaxf(__builtin_fmaxf((a), (b)), (c))

struct AttnState {
  f32x16 o[4];
  f32x16 negm;
  f32x16 s[2][2];
  u32x4 pk[4];
  bf16x8 kf[4];
  s16x4 vlo[4], vhi[4];
  float mref, l;
};

template <int T, int M> DI void k1_kread(AttnState& st, lds_cptr (&kb_)[4]) {
  st.kf[M & 3] = lds_rd128(kb_[M & 3] + k_off(T) + ((M & 7) >> 2) * 8192 + (M >> 3) * 4096);
}
template <int T, int M> DI void k1_vread(AttnState& st, lds_cptr (&vb_)[4]) {
  st.vlo[M & 3] = lds_rdtr(vb_[M & 3] + v_off(T) + (M >> 2) * 4096);
  st.vhi[M & 3] = lds_rdtr(vb_[M & 3] + v_off(T) + (M >> 2) * 4096 + 2048);
}
template <int M> DI void k1_vread_last(AttnState& st, lds_cptr (&vb_)[4]) {
  st.vlo[M & 3] = lds_rdtr(vb_[M >> 2] + v_off(KV_TILES - 1) + (M & 3) * 4096);
  st.vhi[M & 3] = lds_rdtr(vb_[M >> 2] + v_off(KV_TILES - 1) + (M & 3) * 4096 + 2048);
}
template <int J> DI void k1_bar_b(const K1Ctx& c) {
  wait_vm<b_wait(J)>();
  if constexpr (J == 0) bar_lgkm();
  else asm volatile("s_barrier" ::: "memory");
}

template <int T, int M>
DI void k1_gap_qk(const K1Ctx& c, AttnState& st, const bf16x8 (&q)[8], lds_cptr (&kb_)[4], lds_cptr (&vb_)[4], float& sacc, float& rm) {
  constexpr int X = T & 1, Y = X ^ 1, kb = M >> 3, ds = M & 7;
  const bf16x8 kf = st.kf[M & 3];
  if constexpr (ds == 0) {
    if constexpr (T == 0) { f32x16 z = {}; st.s[X][kb] = MFMA(kf, q[ds], z); }
    else st.s[X][kb] = MFMA(kf, q[ds], st.negm);
  } else st.s[X][kb] = MFMA(kf, q[ds], st.s[X][kb]);
  if constexpr (M + 4 < 16) k1_kread<T, M + 4>(st, kb_);
  if constexpr (T > 0) {
    if constexpr (M >= 12) k1_vread<T - 1, M - 12>(st, vb_);
    const f32x16& P = st.s[Y][M >> 3];
    constexpr int e = (M & 7) * 2;
    sacc += P[e]; sacc += P[e + 1];
    st.pk[M >> 2][M & 3] = cvtpk(P[e], P[e + 1]);
    PIN(sacc); PIN(st.pk[M >> 2]);
  }
  if constexpr (M >= 10 && M < 14) {
    constexpr int e = (M - 10) * 4;
    const f32x16& S0 = st.s[X][0];
    if constexpr (M == 10) rm = MX3(S0[0], S0[1], S0[2]); else rm = MX3(rm, S0[e - 1], S0[e]);
    if constexpr (M == 10) rm = fmaxf(rm, S0[3]);
    else rm = MX3(rm, S0[e + 1], S0[e + 2]);
    if constexpr (M == 13) rm = fmaxf(rm, S0[15]);
    PIN(rm);
  }
  SBAR();
  if constexpr (T == 0) { if constexpr ((M & 1) == 0) k1_issue_b1<0, (M >> 1)>(c); }
  else if constexpr ((M & 3) == 2) k1_issue_b1<T, 4 + (M >> 2)>(c);
  if constexpr (T == 6 && (M & 3) == 1) { k2_issue_w<2 + (M >> 3), ((M >> 2) & 1)>(c); SBAR(); }
  if constexpr (T == 7 && (M & 7) == 1) { k2_issue_w<5, (M >> 3)>(c); SBAR(); }
}
template <int T, int... Ms>
DI void k1_qk(const K1Ctx& c, AttnState& st, const bf16x8 (&q)[8], lds_cptr (&kb_)[4], lds_cptr (&vb_)[4], float& sacc, float& rm,
              std::integer_sequence<int, Ms...>) {
  (k1_gap_qk<T, Ms>(c, st, q, kb_, vb_, sacc, rm), ...);
}
template <int T, int M, int E0, int E1>
DI void k1_gap_pv(const K1Ctx& c, AttnState& st, lds_cptr (&kb_)[4], lds_cptr (&vb_)[4]) {
  constexpr int X = T & 1;
  st.o[M & 3] = MFMA(cat8(st.vlo[M & 3], st.vhi[M & 3]), __builtin_bit_cast(bf16x8, st.pk[M >> 2]), st.o[M & 3]);
  if constexpr (M + 4 < 16) k1_vread<T - 1, M + 4>(st, vb_);
  if constexpr (M >= 12) {
    if constexpr (T + 1 < KV_TILES) k1_kread<T + 1, M - 12>(st, kb_);
    else k1_vread_last<M - 12>(st, vb_);
  }
#pragma unroll
  for (int e = E0; e < E1; ++e) st.s[X][e >> 4][e & 15] = __builtin_amdgcn_exp2f(st.s[X][e >> 4][e & 15]);
  if constexpr (E0 < E1) { PIN(st.s[X][E0 >> 4]); if constexpr (((E1 - 1) >> 4) != (E0 >> 4)) PIN(st.s[X][(E1 - 1) >> 4]); }
  SBAR();
  if constexpr ((M & 3) == 2) k1_issue_b1<T + 1, (M >> 2)>(c);
  if constexpr (T >= WO_STEP && T < WO_STEP + 5 && M == 7) k1_touch_wo<T - WO_STEP>(c);
  if constexpr (T == 5 && (M & 3) == 1) { k2_issue_w<(M >> 3), ((M >> 2) & 1)>(c); SBAR(); }
  if constexpr (T == 6 && (M & 7) == 1) { k2_issue_w<4, (M >> 3)>(c); SBAR(); }
  if constexpr (T == 7 && (M & 3) == 1) { k2_issue_w<6 + (M >> 3), ((M >> 2) & 1)>(c); SBAR(); }
}
constexpr int pv_e(int m) { return m < 2 ? 0 : m >= 16 ? 32 : ((m - 2) * 32 + 6) / 14; }
template <int T, int... Ms>
DI void k1_pv_tail(const K1Ctx& c, AttnState& st, lds_cptr (&kb_)[4], lds_cptr (&vb_)[4], std::integer_sequence<int, Ms...>) {
  (k1_gap_pv<T, Ms + 2, pv_e(Ms + 2), pv_e(Ms + 3)>(c, st, kb_, vb_), ...);
}

template <int T>
DI void k1_step_b(const K1Ctx& c, AttnState& st, const bf16x8 (&q)[8], lds_cptr (&kb_)[4], lds_cptr (&vb_)[4]) {
  constexpr int X = T & 1;
  float sacc = 0.f, rm = 0.f;
  if constexpr (T == 5) {
    const unsigned l = fresh_lane();
    c.vwo = (l >> 3) * (DM * 2) + (((l & 7) ^ (((l >> 4) & 1) << 2)) << 4);
  }
  k1_qk<T>(c, st, q, kb_, vb_, sacc, rm, std::make_integer_sequence<int, 16>{});
  st.l += sacc;
  k1_bar_b<T + 1>(c);
  if constexpr (T == 0) {
    k1_issue_b1<1, 0>(c); k1_issue_b1<1, 1>(c); k1_issue_b1<1, 2>(c); k1_issue_b1<1, 3>(c);
    k1_kread<1, 0>(st, kb_); k1_kread<1, 1>(st, kb_); k1_kread<1, 2>(st, kb_); k1_kread<1, 3>(st, kb_);
#pragma unroll
    for (int r = 0; r < 16; r += 2) rm = MX3(rm, st.s[X][1][r], st.s[X][1][r + 1]);
    rm = half_max(rm);
    st.mref = rm;
#pragma unroll
    for (int r = 0; r < 16; ++r) {
      st.s[X][0][r] = __builtin_amdgcn_exp2f(st.s[X][0][r] - rm);
      st.s[X][1][r] = __builtin_amdgcn_exp2f(st.s[X][1][r] - rm);
      st.negm[r] = -rm;
    }
    SBAR();
  } else {
    k1_gap_pv<T, 0, 0, 0>(c, st, kb_, vb_);
#pragma unroll
    for (int r = 0; r < 16; r += 2) rm = MX3(rm, st.s[X][1][r], st.s[X][1][r + 1]);
    PIN(rm);
    k1_gap_pv<T, 1, 0, 0>(c, st, kb_, vb_);
    rm = half_max(rm);
    bool resc = false;
    float f = 1.f;
    if (__builtin_expect(__any(rm > THR), 0)) {
      const float dl = fmaxf(rm, 0.f);
      st.mref += dl;
      f = __builtin_amdgcn_exp2f(-dl);
      st.l *= f;
#pragma unroll
      for (int r = 0; r < 16; ++r) { st.s[X][0][r] -= dl; st.s[X][1][r] -= dl; st.negm[r] = -st.mref; }
      resc = true;
    }
    SBAR();
    k1_pv_tail<T>(c, st, kb_, vb_, std::make_integer_sequence<int, 14>{});
    if (__builtin_expect(resc, 0)) {
#pragma unroll
      for (int dt = 0; dt < 4; ++dt)
#pragma unroll
        for (int r = 0; r < 16; ++r) st.o[dt][r] *= f;
    }
    SBAR();
  }
}
struct OStore { __amdgpu_buffer_rsrc_t srd; unsigned off; float inv; };
template <int DT> DI void k1_store_o(AttnState& st, const OStore& os) {
#pragma unroll
  for (int i = 0; i < 16; ++i) st.o[DT][i] *= os.inv;
  __builtin_amdgcn_raw_buffer_store_b128(__builtin_bit_cast(u32x4, pack8(st.o[DT], 0)), os.srd, os.off + (2 * DT) * 1024, 0, 16);
  __builtin_amdgcn_raw_buffer_store_b128(__builtin_bit_cast(u32x4, pack8(st.o[DT], 1)), os.srd, os.off + (2 * DT + 1) * 1024, 0, 16);
}
template <int M> DI void k1_gap_last(AttnState& st, lds_cptr (&vb_)[4], const OStore& os) {
  st.o[M >> 2] = MFMA(cat8(st.vlo[M & 3], st.vhi[M & 3]), __builtin_bit_cast(bf16x8, st.pk[M & 3]), st.o[M >> 2]);
  if constexpr (M + 4 < 16) k1_vread_last<M + 4>(st, vb_);
  if constexpr (M >= 4 && (M & 3) == 1) k1_store_o<(M >> 2) - 1>(st, os);
  SBAR();
}
template <int... Ms> DI void k1_last(AttnState& st, lds_cptr (&vb_)[4], OStore& os, std::integer_sequence<int, Ms...>) {
  constexpr int X = (KV_TILES - 1) & 1;
  float sacc = 0.f;
#pragma unroll
  for (int kb = 0; kb < 2; ++kb)
#pragma unroll
    for (int e = 0; e < 16; e += 2) {
      sacc += st.s[X][kb][e]; sacc += st.s[X][kb][e + 1];
      st.pk[kb * 2 + (e >> 3)][(e >> 1) & 3] = cvtpk(st.s[X][kb][e], st.s[X][kb][e + 1]);
    }
  os.inv = 1.0f / half_sum(st.l + sacc);
  SBAR();
  (k1_gap_last<Ms>(st, vb_, os), ...);
  k1_store_o<3>(st, os);
}
template <int... Is>
DI void k1_phase_b(const K1Ctx& c, AttnState& st, const bf16x8 (&q)[8], lds_cptr (&kb_)[4], lds_cptr (&vb_)[4],
                   std::integer_sequence<int, Is...>) {
  k1_bar_b<0>(c);
  k1_kread<0, 0>(st, kb_); k1_kread<0, 1>(st, kb_); k1_kread<0, 2>(st, kb_); k1_kread<0, 3>(st, kb_);
  SBAR();
  (k1_step_b<Is>(c, st, q, kb_, vb_), ...);
}

constexpr int K2_W = 4;
constexpr int k2_seq(int j) { return j < 8 ? 16 + j : j < 24 ? j - 8 : j; }
constexpr int k2_ops(int t) { return (t < 8 ? 2 : 0) + (t + K2_W < 32 ? 2 : 0); }
constexpr int k2_wait(int j) {
  int n = j < K2_W ? 2 * (K2_W - 1 - j) : 0;
  for (int t = (j < K2_W ? 0 : j - K2_W + 1); t < j; ++t) n += k2_ops(t);
  return n;
}
struct K2State {
  __amdgpu_buffer_rsrc_t so;
  unsigned va;
  bf16x8 wr[16][2];
  u32x4 ow[K2_W][2];
  f32x16 acc[2][2];
};
template <int J> DI void k2_load_o(const K1Ctx& c, K2State& k) {
  if constexpr (J < 32) {
    const unsigned so = (32 * c.wid + k2_seq(J)) * 1024;
    asm volatile("buffer_load_dwordx4 %0, %1, %2, %3 offen sc1" : "=v"(k.ow[J % K2_W][0]) : "v"(k.va), "s"(k.so), "s"(so) : "memory");
    asm volatile("buffer_load_dwordx4 %0, %1, %2, %3 offen sc1" : "=v"(k.ow[J % K2_W][1]) : "v"(k.va), "s"(k.so), "s"(so + 262144) : "memory");
  }
}
template <int I> DI void k2_read_w(bf16x8 (&wf)[2], lds_cptr (&bb)[2]) {
#pragma unroll
  for (int nb = 0; nb < 2; ++nb) wf[nb] = cat8(lds_rdtr(bb[nb] + w_off(I & 7)), lds_rdtr(bb[nb] + w_off(I & 7) + 1024));
}
template <int G, int... Is> DI void k2_park_w(K2State& k, lds_cptr (&bb)[2], std::integer_sequence<int, Is...>) {
  (k2_read_w<8 * G + Is>(k.wr[8 * G + Is], bb), ...);
}
template <int G, int J, int N> DI void k2_swap_w(const K1Ctx& c, K2State& k, lds_cptr (&bb)[2]) {
  wait_vm<N>();
  k2_read_w<8 * G + J>(k.wr[8 * G + J], bb);
  wait_lgkm0();
  SBAR();
  k2_issue_w<8 * (G + 1) + J, 0>(c); k2_issue_w<8 * (G + 1) + J, 1>(c);
}
template <int J> DI void k2_step(const K1Ctx& c, K2State& k, lds_cptr (&bb)[2]) {
  constexpr int I = k2_seq(J);
  asm volatile("s_waitcnt vmcnt(%2)" : "+v"(k.ow[J % K2_W][0]), "+v"(k.ow[J % K2_W][1]) : "i"(k2_wait(J)) : "memory");
  bf16x8 wf[2];
  if constexpr (I >= 16) k2_read_w<I>(wf, bb);
  else { wf[0] = k.wr[I][0]; wf[1] = k.wr[I][1]; }
#pragma unroll
  for (int nb = 0; nb < 2; ++nb)
#pragma unroll
    for (int mb = 0; mb < 2; ++mb) k.acc[nb][mb] = MFMA(wf[nb], __builtin_bit_cast(bf16x8, k.ow[J % K2_W][mb]), k.acc[nb][mb]);
  SBAR();
  if constexpr (J < 8) { wait_lgkm0(); k2_issue_w<I + 8, 0>(c); k2_issue_w<I + 8, 1>(c); }
  k2_load_o<J + K2_W>(c, k);
  SBAR();
}
template <int... Js> DI void k2_loop(const K1Ctx& c, K2State& k, lds_cptr (&bb)[2], std::integer_sequence<int, Js...>) {
  (k2_step<Js>(c, k, bb), ...);
}

extern "C" __device__ unsigned long long llvm_dispatch_id() __asm("llvm.amdgcn.dispatch.id");
typedef __attribute__((address_space(1))) unsigned long long gu64;
constexpr size_t OFRAG_BYTES = (size_t)2 * 28 * 256 * 1024;
constexpr unsigned SPIN_CAP = 1u << 16;

__global__ __launch_bounds__(512, 2) void k1_attn(const __hip_bfloat16* __restrict__ xA, const __hip_bfloat16* __restrict__ kA,
                                                  const __hip_bfloat16* __restrict__ vA, const __hip_bfloat16* __restrict__ wqA,
                                                  const __hip_bfloat16* __restrict__ xB, const __hip_bfloat16* __restrict__ kB,
                                                  const __hip_bfloat16* __restrict__ vB, const __hip_bfloat16* __restrict__ wqB,
                                                  const __hip_bfloat16* __restrict__ woA, const __hip_bfloat16* __restrict__ woB,
                                                  u32x4* ofrag, __hip_bfloat16* __restrict__ out) {
  extern __shared__ __attribute__((aligned(16))) char lds[];
  const int tid = threadIdx.x, lane = tid & 63;
  const unsigned wid = __builtin_amdgcn_readfirstlane(tid >> 6);
  const int bid = blockIdx.x, xcd = bid & 7, idx = bid >> 3;
  const int p = xcd >> 2, kvh = (xcd & 3) * 4 + (idx & 3), b = idx >> 2;
  const int hh = wid >> 2, rb = wid & 3;
  const int r = lane & 31, h = lane >> 5, blk = (lane >> 4) & 1, qq = (lane & 15) >> 2, pp = lane & 3;

  unsigned long long tag = llvm_dispatch_id() * 0x9E3779B97F4A7C15ull + (unsigned long long)(uintptr_t)__builtin_amdgcn_dispatch_ptr() * 0xBF58476D1CE4E5B9ull;
  tag = ((tag ^ (tag >> 29)) & ~0xFFFFull) | 0xA55Aull;
  gu64* flags = (gu64*)((char*)ofrag + OFRAG_BYTES);
  int fidx = (p * NB + b) * HKV + kvh;
  asm volatile("" : "+s"(fidx));
  gu64* myflag = flags + fidx;

  K1Ctx c;
  c.ldsb = (unsigned)(uintptr_t)lds;
  c.wid = wid;
  {
    const __hip_bfloat16* x = p ? xB : xA; const __hip_bfloat16* wq = p ? wqB : wqA;
    const __hip_bfloat16* kc = p ? kB : kA; const __hip_bfloat16* vc = p ? vB : vA;
    c.sx = make_srd(x + (size_t)b * SQ * DM, SQ * DM * 2);
    c.sw = make_srd(wq + kvh * 256, (DM * NQ - kvh * 256) * 2);
    c.sk = make_srd(kc + (size_t)(b * HKV + kvh) * SK * DH, SK * DH * 2);
    c.sv = make_srd(vc + (size_t)(b * HKV + kvh) * SK * DH, SK * DH * 2);
  }
  c.swo = make_srd(p ? woB : woA, NQ * DM * 2);
  const int mh = (xcd >> 1) & 1, nh = xcd & 1;
  const bool has_tile = nh * 32 + idx < 63;
  const int tile = has_tile ? nh * 32 + idx : 62;
  const int nt = tile / 7, mt = 7 * mh + (tile - nt * 7);
  c.wo_off = nh * 512;
  c.wo_q = idx;
  c.swt = make_srd((p ? woB : woA) + nt * 64, (NQ * DM - nt * 64) * 2);
  const unsigned wrel = wid * 2048;
  c.wbuf = c.ldsb + wrel;

  c.warm2 = 0;
  c.grp = idx & 3;
  {
    const unsigned xr = 16 * wid + (lane >> 2);
    c.vx = xr * (DM * 2) + (((lane & 3) ^ ((xr >> 2) & 3)) << 4);
    const unsigned k0 = 4 * wid + (lane >> 5), k1 = k0 + 2;
    c.vw0 = k0 * 8192 + (((lane & 31) ^ ((k0 & 3) << 2)) << 4);
    c.vw1 = k1 * 8192 + (((lane & 31) ^ ((k1 & 3) << 2)) << 4);
    k1_issue_a<0>(c); k1_issue_a<1>(c); k1_issue_a<2>(c); k1_issue_a<3>(c);
    const unsigned kr = 8 * wid + (lane >> 3), vr = 8 * wid + (lane >> 4);
    c.vk = kr * 256 + (((lane & 7) ^ ((kr >> 1) & 7)) << 4);
    c.vv = vr * 256 + (((lane & 15) ^ ((vr & 3) << 2)) << 4);
  }

  if (tid == 448) __hip_atomic_store(myflag, 0ull, __ATOMIC_RELAXED, __HIP_MEMORY_SCOPE_AGENT);

  const lds_cptr L = (lds_cptr)lds;
  const int xrow = rb * 32 + r;
  lds_cptr xb[2];
  xb[0] = L + xrow * 64 + ((h ^ ((xrow >> 2) & 3)) << 4);
  xb[1] = L + xrow * 64 + (((2 + h) ^ ((xrow >> 2) & 3)) << 4);
  lds_cptr wb[4];
  {
    const int psw = ((pp & 1) << 1) | (pp >> 1);
#pragma unroll
    for (int dt = 0; dt < 4; ++dt) wb[dt] = L + (8 * h + qq) * 512 + hh * 256 + ((dt ^ qq) << 6) + blk * 32 + psw * 8;
  }
  f32x16 qa[4] = {};
  k1_phase_a(c, qa, xb, wb, std::make_integer_sequence<int, A_STAGES>{});

  bf16x8 q[8];
#pragma unroll
  for (int dt = 0; dt < 4; ++dt) {
#pragma unroll
    for (int i = 0; i < 16; ++i) qa[dt][i] *= QSCALE;
    q[2 * dt] = pack8(qa[dt], 0);
    q[2 * dt + 1] = pack8(qa[dt], 1);
  }

  lds_cptr kb_[4], vb_[4];
#pragma unroll
  for (int j = 0; j < 4; ++j) {
    kb_[j] = L + r * 128 + (((2 * j + h) ^ ((r >> 1) & 7)) << 4);
    vb_[j] = L + (4 * h + qq) * 256 + ((j ^ qq) << 6) + blk * 32 + pp * 8;
  }
  AttnState st;
#pragma unroll
  for (int dt = 0; dt < 4; ++dt) st.o[dt] = f32x16{};
  st.negm = f32x16{}; st.mref = 0.f; st.l = 0.f;
  k1_phase_b(c, st, q, kb_, vb_, std::make_integer_sequence<int, KV_TILES>{});

  const int lane2 = fresh_lane();
  const int r2 = lane2 & 31, h2 = lane2 >> 5, blk2 = (lane2 >> 4) & 1, qq2 = (lane2 & 15) >> 2, pp2 = lane2 & 3;
  {
    OStore os;
    os.srd = make_srd(ofrag, (unsigned)OFRAG_BYTES);
    const int xrow2 = rb * 32 + r2, g = b * SQ + xrow2, head = kvh * 2 + hh;
    const unsigned off = (unsigned)(((p * 28 + (g >> 5)) * 256 + head * 8) * 64 + h2 * 32 + (g & 31)) * 16u;
    os.off = xrow2 < SQ ? off : 0x80000000u;
    k1_last(st, vb_, os, std::make_integer_sequence<int, 16>{});
  }
  K2State k;
  lds_cptr bb[2];
#pragma unroll
  for (int nb = 0; nb < 2; ++nb) bb[nb] = L + wrel + (4 * h2 + qq2) * 128 + ((nb ^ ((qq2 >> 1) & 1)) << 6) + blk2 * 32 + pp2 * 8;
  asm volatile("s_waitcnt vmcnt(8)" :: "v"(c.warm2) : "memory");
  k2_park_w<0>(k, bb, std::make_integer_sequence<int, 8>{});
  wait_lgkm0();
  SBAR();
  asm volatile("s_waitcnt vmcnt(0)\n\ts_barrier" ::: "memory");
  if (wid == 7 && lane2 == 0) __hip_atomic_store(myflag, tag, __ATOMIC_RELAXED, __HIP_MEMORY_SCOPE_AGENT);
  if (has_tile) {
    k2_issue_wgroup<1>(c);
    const int b0 = (64 * mt) / SQ, b1 = (64 * mt + 63) / SQ;
    const int sel = lane2 < 2 * (b1 - b0 + 1) ? lane2 : 0;
    gu64* fp = flags + (p * NB + b0 + (sel >> 1)) * HKV + 2 * wid + (sel & 1);
    unsigned long long pv;
    k2_swap_w<1, 0, 14>(c, k, bb); k2_swap_w<1, 1, 14>(c, k, bb); k2_swap_w<1, 2, 14>(c, k, bb); k2_swap_w<1, 3, 14>(c, k, bb);
    k2_swap_w<1, 4, 14>(c, k, bb); k2_swap_w<1, 5, 14>(c, k, bb);
    asm volatile("global_load_dwordx2 %0, %1, off sc1" : "=v"(pv) : "v"(fp) : "memory");
    k2_swap_w<1, 6, 14>(c, k, bb); k2_swap_w<1, 7, 14>(c, k, bb);
    k.so = make_srd(ofrag + (size_t)((p * 28 + 2 * mt) * 256) * 64, 2 * 256 * 1024);
    k.va = lane2 * 16;
    asm volatile("s_waitcnt vmcnt(4)" : "+v"(pv) :: "memory");
    if (!__all(pv == tag)) {
      for (unsigned spins = 0; spins < SPIN_CAP; ++spins) {
        const unsigned long long v = __hip_atomic_load(fp, __ATOMIC_RELAXED, __HIP_MEMORY_SCOPE_AGENT);
        if (__all(v == tag)) break;
        __builtin_amdgcn_s_sleep(2);
      }
    }
    __builtin_amdgcn_fence(__ATOMIC_ACQUIRE, "wavefront");
#pragma unroll
    for (int nb = 0; nb < 2; ++nb)
#pragma unroll
      for (int mb = 0; mb < 2; ++mb) k.acc[nb][mb] = f32x16{};
    k2_load_o<0>(c, k); k2_load_o<1>(c, k); k2_load_o<2>(c, k); k2_load_o<3>(c, k);
    SBAR();
    k2_loop(c, k, bb, std::make_integer_sequence<int, 32>{});

    const int lane3 = fresh_lane();
#pragma unroll
    for (int Q = 0; Q < 16; ++Q) {
      const int nb = Q >> 3, mb = (Q >> 2) & 1, g = Q & 3;
      const f32x4 v = {k.acc[nb][mb][4 * g], k.acc[nb][mb][4 * g + 1], k.acc[nb][mb][4 * g + 2], k.acc[nb][mb][4 * g + 3]};
      *(f32x4*)(lds + w_off(Q >> 1) + wrel + (Q & 1) * 1024 + lane3 * 16) = v;
    }
    __syncthreads();
    const int rh = wid == 0 ? 9 : wid < 4 ? 3 + wid : wid == 4 ? 2 : wid == 5 ? 0 : wid == 6 ? 1 : 7;
    const char* rsrc = lds + rh * 16384 + lane3 * 16;
#pragma unroll
    for (int j = 0; j < 2; ++j) {
      const int Q = wid * 2 + j;
      f32x4 sm = *(const f32x4*)(rsrc + j * 1024);
#pragma unroll
      for (int w = 1; w < 8; ++w) sm += *(const f32x4*)(rsrc + w * 2048 + j * 1024);
      const int nb = Q >> 3, mb = (Q >> 2) & 1, g = Q & 3;
      const int row = mt * 64 + mb * 32 + (lane3 & 31);
      const int n = nt * 64 + nb * 32 + 8 * g + 4 * (lane3 >> 5);
      u32x2 o = {cvtpk(sm[0], sm[1]), cvtpk(sm[2], sm[3])};
      *(u32x2*)(out + (size_t)row * (2 * DM) + p * DM + n) = o;
    }
  }
}

extern "C" void kernel_launch(void* const* d_in, const int* in_sizes, int n_in,
                              void* d_out, int out_size, void* d_ws, size_t ws_size,
                              hipStream_t stream) {
  const __hip_bfloat16* xA = (const __hip_bfloat16*)d_in[0];
  const __hip_bfloat16* kA = (const __hip_bfloat16*)d_in[1];
  const __hip_bfloat16* vA = (const __hip_bfloat16*)d_in[2];
  const __hip_bfloat16* wqA = (const __hip_bfloat16*)d_in[3];
  const __hip_bfloat16* woA = (const __hip_bfloat16*)d_in[4];
  const __hip_bfloat16* xB = (const __hip_bfloat16*)d_in[5];
  const __hip_bfloat16* kB = (const __hip_bfloat16*)d_in[6];
  const __hip_bfloat16* vB = (const __hip_bfloat16*)d_in[7];
  const __hip_bfloat16* wqB = (const __hip_bfloat16*)d_in[8];
  const __hip_bfloat16* woB = (const __hip_bfloat16*)d_in[9];
  u32x4* ofrag = (u32x4*)d_ws;
  (void)hipFuncSetAttribute((const void*)k1_attn, hipFuncAttributeMaxDynamicSharedMemorySize, K1_LDS);
  hipLaunchKernelGGL(k1_attn, dim3(256), dim3(512), K1_LDS, stream, xA, kA, vA, wqA, xB, kB, vB, wqB, woA, woB, ofrag, (__hip_bfloat16*)d_out);
}
```
